```python
import torch

def _softmax(x, axis=-1):
    return torch.softmax(x, dim=axis)
def _gelu(x):
    return torch.nn.functional.gelu(x, approximate="tanh")
def _silu(x):
    return torch.nn.functional.silu(x)
def _sigmoid(x):
    return torch.sigmoid(x)
def _layernorm(x, axis=-1, eps=1e-5):
    m = x.mean(dim=axis, keepdim=True)
    v = x.var(dim=axis, keepdim=True, unbiased=False)
    return (x - m) / torch.sqrt(v + eps)
def _scatter_add(tgt, idx, src, axis=0):
    return tgt.clone().index_add_(axis, idx, src)
def _segment_sum(src, seg, n):
    out = torch.zeros((n,) + src.shape[1:], dtype=src.dtype)
    return out.index_add_(0, seg, src)
def _pad(x, pw):
    flat = tuple(v for pair in reversed(pw) for v in pair)
    return torch.nn.functional.pad(x, flat)

kernel_name = 'parallel_sum__ssm_conv_h3__geglu_ffn__prompt'

def setup_inputs():

    _w = (lambda a: (lambda t: t.to(torch.bfloat16) if t.is_floating_point() else t)(torch.as_tensor(a)))
    def _A_setup():
        B, S, D = 1, 2048, 512
        ks = 4
        return {
            "q": _w((lambda *s: torch.randn(*s, dtype=torch.bfloat16) * 0.05)(B, S, D)),
            "k": _w((lambda *s: torch.randn(*s, dtype=torch.bfloat16) * 0.8)(B, S, D)),
            "v": _w((lambda *s: torch.randn(*s, dtype=torch.bfloat16) * 0.8)(B, S, D)),
            "shift_k": _w((lambda *s: torch.randn(*s, dtype=torch.bfloat16) * 0.8)(D, ks) * 0.5),
            "ssm_k": _w((lambda *s: torch.randn(*s, dtype=torch.bfloat16) * 0.8)(D, S) * (1.0 / S ** 0.5)),
            "ssm_D": _w((lambda *s: torch.randn(*s, dtype=torch.bfloat16) * 0.8)(D)),
        }
    _A_in = _A_setup()

    def _B_setup():
        B, S, D, F = 1, 2048, 512, 1376
        return {
            "x": _w((lambda *s: torch.randn(*s, dtype=torch.bfloat16) * 0.05)(B, S, D)),
            "w_gate": _w((lambda *s: torch.randn(*s, dtype=torch.bfloat16) * 0.05)(D, F)),
            "w_up": _w((lambda *s: torch.randn(*s, dtype=torch.bfloat16) * 0.05)(D, F)),
            "w_down": _w((lambda *s: torch.randn(*s, dtype=torch.bfloat16) * 0.05)(F, D)),
        }
    _B_in = _B_setup()

    return {
        "A_q": _A_in["q"],
        "A_k": _A_in["k"],
        "A_v": _A_in["v"],
        "A_shift_k": _A_in["shift_k"],
        "A_ssm_k": _A_in["ssm_k"],
        "A_ssm_D": _A_in["ssm_D"],
        "B_x": _B_in["x"],
        "B_w_gate": _B_in["w_gate"],
        "B_w_up": _B_in["w_up"],
        "B_w_down": _B_in["w_down"],
    }

def reference(A_q, A_k, A_v, A_shift_k, A_ssm_k, A_ssm_D, B_x, B_w_gate, B_w_up, B_w_down):
    def _A_ref(q, k, v, shift_k, ssm_k, ssm_D):
        B, S, D = q.shape
        ks = shift_k.shape[-1]
        k_pad = _pad(k, ((0, 0), (ks - 1, 0), (0, 0)))
        k_shift = torch.zeros_like(k)
        for t in range(ks):
            k_shift = k_shift + k_pad[:, t:t + S, :] * shift_k[:, ks - 1 - t][None, None, :]
        kv = k_shift * v
        L = 2 * S
        kv_pad = _pad(kv, ((0, 0), (0, S), (0, 0)))
        kf_pad = _pad(ssm_k, ((0, 0), (0, S)))
        kv_f = (lambda x, axes=(-2,-1): torch.fft.rfft2(x.float(), dim=axes))(kv_pad[:, None, :, :], axes=(1, 2))
        kf_f = (lambda x, axes=(-2,-1): torch.fft.rfft2(x.float(), dim=axes))(kf_pad[:, None, :], axes=(1, 2))
        kf_f = (lambda x, *d: x.permute(*d))(kf_f, 1, 2, 0)
        y_f = kv_f * kf_f[None, :, :, :]
        y = (lambda x, s, axes=(-2,-1): torch.fft.irfft2(x, s=s, dim=axes).to(torch.bfloat16))(y_f, (1, L), axes=(1, 2))[:, 0, :S, :]
        y = y + kv * ssm_D[None, None, :]
        return q * y

    def _B_ref(x, w_gate, w_up, w_down):
        return (_gelu(x @ w_gate) * (x @ w_up)) @ w_down

    x = A_q
    xn = _layernorm(x, axis=-1)
    return x + _A_ref(xn, A_k, A_v, A_shift_k, A_ssm_k, A_ssm_D) + _B_ref(xn, B_w_gate, B_w_up, B_w_down)
```

```cpp
#include <hip/hip_runtime.h>
#include <hip/hip_bf16.h>
#include <stdint.h>

typedef unsigned short u16;
__device__ __forceinline__ float bf2f(u16 b){ return __uint_as_float(((unsigned)b)<<16); }
__device__ __forceinline__ u16 f2bf(float f){ unsigned u=__float_as_uint(f); u += 0x7FFFu + ((u>>16)&1u); return (u16)(u>>16); }
__device__ __forceinline__ float rbf(float f){ return bf2f(f2bf(f)); }

constexpr int S=2048, D=512, F=1376;

__global__ void k_pre(const u16* x,const u16* k,const u16* v,const u16* sk,u16* xn,u16* kv){
  int s=blockIdx.x; int d=threadIdx.x;
  __shared__ float red[512];
  float xv=bf2f(x[s*D+d]);
  red[d]=xv; __syncthreads();
  for(int o=256;o>0;o>>=1){ if(d<o) red[d]+=red[d+o]; __syncthreads(); }
  float mean=red[0]/D; __syncthreads();
  float df=xv-mean; red[d]=df*df; __syncthreads();
  for(int o=256;o>0;o>>=1){ if(d<o) red[d]+=red[d+o]; __syncthreads(); }
  float var=red[0]/D;
  float mb=rbf(mean), vb=rbf(var);
  float sd=rbf(sqrtf(rbf(vb+1e-5f)));
  xn[s*D+d]=f2bf(rbf(xv-mb)/sd);
  float ks=0.f;
  for(int t=0;t<4;++t){ int ss=s-3+t; float kk= ss>=0? bf2f(k[ss*D+d]):0.f; ks=rbf(ks+rbf(kk*bf2f(sk[d*4+3-t]))); }
  kv[s*D+d]=f2bf(ks*bf2f(v[s*D+d]));
}
__global__ void k_conv(const u16* kv,const u16* h,const u16* sD,u16* z){
  int d=blockIdx.x; __shared__ float u[S]; __shared__ float hh[S];
  for(int i=threadIdx.x;i<S;i+=blockDim.x){ u[i]=bf2f(kv[i*D+d]); hh[i]=bf2f(h[d*S+i]); }
  __syncthreads();
  float Dd=bf2f(sD[d]);
  for(int s=threadIdx.x;s<S;s+=blockDim.x){
    float a=0.f; for(int j=0;j<=s;++j) a+=hh[j]*u[s-j];
    z[s*D+d]=f2bf(rbf(a)+rbf(u[s]*Dd));
  }
}
__global__ void k_g1(const u16* xn,const u16* wg,const u16* wu,u16* H){
  int s=blockIdx.x; __shared__ float xr[D];
  for(int i=threadIdx.x;i<D;i+=blockDim.x) xr[i]=bf2f(xn[s*D+i]);
  __syncthreads();
  for(int f=threadIdx.x;f<F;f+=blockDim.x){
    float g=0,u=0; for(int k=0;k<D;++k){ g+=xr[k]*bf2f(wg[k*F+f]); u+=xr[k]*bf2f(wu[k*F+f]); }
    g=rbf(g); u=rbf(u);
    float t=tanhf(0.7978845608028654f*(g+0.044715f*g*g*g));
    float ge=rbf(0.5f*g*(1.f+t));
    H[s*F+f]=f2bf(ge*u);
  }
}
__global__ void k_g2(const u16* H,const u16* wd,const u16* x,const u16* xn,const u16* z,u16* out){
  int s=blockIdx.x; int d=threadIdx.x; __shared__ float hr[F];
  for(int i=threadIdx.x;i<F;i+=blockDim.x) hr[i]=bf2f(H[s*F+i]);
  __syncthreads();
  float a=0; for(int f=0;f<F;++f) a+=hr[f]*bf2f(wd[f*D+d]);
  float A=rbf(bf2f(xn[s*D+d])*bf2f(z[s*D+d]));
  out[s*D+d]=f2bf(rbf(bf2f(x[s*D+d])+A)+rbf(a));
}

extern "C" void kernel_launch(void* const* d_in, const int* in_sizes, int n_in,
                              void* d_out, int out_size, void* d_ws, size_t ws_size,
                              hipStream_t stream) {
  const u16* x=(const u16*)d_in[0]; const u16* k=(const u16*)d_in[1]; const u16* v=(const u16*)d_in[2];
  const u16* sk=(const u16*)d_in[3]; const u16* h=(const u16*)d_in[4]; const u16* sD=(const u16*)d_in[5];
  const u16* wg=(const u16*)d_in[7]; const u16* wu=(const u16*)d_in[8]; const u16* wd=(const u16*)d_in[9];
  u16* ws=(u16*)d_ws; u16* xn=ws; u16* kv=ws+S*D; u16* z=ws+2*S*D; u16* H=ws+3*S*D;
  k_pre<<<S,512,0,stream>>>(x,k,v,sk,xn,kv);
  k_conv<<<D,256,0,stream>>>(kv,h,sD,z);
  k_g1<<<S,256,0,stream>>>(xn,wg,wu,H);
  k_g2<<<S,512,0,stream>>>(H,wd,x,xn,z,(u16*)d_out);
}
```

```cpp
#include <hip/hip_runtime.h>
#include <hip/hip_bf16.h>
#include <stdint.h>

typedef unsigned short u16;
typedef unsigned v4u __attribute__((ext_vector_type(4)));
typedef unsigned v2u __attribute__((ext_vector_type(2)));
typedef short bf16x8 __attribute__((ext_vector_type(8)));
typedef short s16x4 __attribute__((ext_vector_type(4)));
typedef float f32x16 __attribute__((ext_vector_type(16)));
typedef float f32x4 __attribute__((ext_vector_type(4)));
typedef float f32x2 __attribute__((ext_vector_type(2)));
typedef __bf16 bf2 __attribute__((ext_vector_type(2)));
#define LAS __attribute__((address_space(3)))
#define GAS __attribute__((address_space(1)))
#define DI __device__ __forceinline__
typedef __amdgpu_buffer_rsrc_t rsrc_t;

constexpr int S = 2048, D = 512, F = 1376, NFB = 86;
constexpr int WROW = F * 2;
constexpr unsigned WS_EPOCH = 0, WS_PF = 1024, WS_HF = 4096, WS_ZF = 8192;
constexpr unsigned WS_STATS = 16384;
constexpr unsigned WS_CSP = 32768;
constexpr unsigned WS_KVR = 262144;
constexpr unsigned WS_ZT = WS_KVR + 2097152;
constexpr unsigned WS_WDT = WS_ZT + 2097152;
constexpr unsigned WS_HT = WS_WDT + 8 * 86 * 2048;
constexpr unsigned WS_END = WS_HT + 32 * 86 * 2048;
constexpr unsigned WS_STAMP = WS_END;
constexpr int SLOT = 40960, L_XW = 16384, L_UP = 28672;
constexpr int L_CS = 122880, L_ST = L_CS + 768, L_CTL = L_CS + 1792;
constexpr int HC_P = 4160, HC_B = 8 * HC_P;
constexpr int L_HC0 = 124928, L_UR0 = L_HC0 + HC_B, L_HC1 = 2 * SLOT, L_UR1 = L_HC1 + HC_B;
constexpr int L_RED = 0, L_ZT = 65536, ZT_P = 192;
constexpr int LDS_BYTES = 163840;
static_assert(L_UR0 + 4608 <= LDS_BYTES && L_UR1 + 4608 <= 3 * SLOT, "lds");
constexpr int SC1 = 16;
constexpr unsigned OOB = 0x80000000u;
constexpr unsigned SPIN_CAP = 1u << 14;

#define BAR() asm volatile("s_waitcnt lgkmcnt(0)\n\ts_barrier" ::: "memory")
#define VMW(n) asm volatile("s_waitcnt vmcnt(%0)" ::"i"(n) : "memory")
#define RLX_AGENT __ATOMIC_RELAXED, __HIP_MEMORY_SCOPE_AGENT

#define STAMP(i)

DI float bflo(unsigned u) { return __uint_as_float(u << 16); }
DI float bfhi(unsigned u) { return __uint_as_float(u & 0xffff0000u); }
DI unsigned pk2(float a, float b) { f32x2 v = {a, b}; return __builtin_bit_cast(unsigned, __builtin_convertvector(v, bf2)); }
DI float rbf(float a) { return bflo(pk2(a, a)); }
DI rsrc_t mkrs(const void* p, unsigned n) { return __builtin_amdgcn_make_buffer_rsrc((void*)p, 0, n, 0x00020000); }
DI v4u ld128(rsrc_t r, unsigned vo, unsigned so, int aux) { return aux ? __builtin_amdgcn_raw_buffer_load_b128(r, vo, so, SC1) : __builtin_amdgcn_raw_buffer_load_b128(r, vo, so, 0); }
DI unsigned ld32(rsrc_t r, unsigned vo, unsigned so) { return __builtin_amdgcn_raw_buffer_load_b32(r, vo, so, 0); }
DI void st128(v4u v, rsrc_t r, unsigned vo, unsigned so) { __builtin_amdgcn_raw_buffer_store_b128(v, r, vo, so, SC1); }
DI void st64(v2u v, rsrc_t r, unsigned vo, unsigned so) { __builtin_amdgcn_raw_buffer_store_b64(v, r, vo, so, SC1); }
DI void dma(rsrc_t r, LAS char* dst, unsigned vo, unsigned so) { __builtin_amdgcn_raw_ptr_buffer_load_lds(r, (LAS void*)dst, 16, vo, so, 0, 0); }
DI bf16x8 asb(v4u v) { return __builtin_bit_cast(bf16x8, v); }
DI f32x16 mfma32(bf16x8 a, bf16x8 b, f32x16 c) { return __builtin_amdgcn_mfma_f32_32x32x16_bf16(a, b, c, 0, 0, 0); }
DI f32x4 mfma16(bf16x8 a, bf16x8 b, f32x4 c) { return __builtin_amdgcn_mfma_f32_16x16x32_bf16(a, b, c, 0, 0, 0); }
DI void flag_set(char* ws, unsigned off, unsigned T) { __hip_atomic_store((GAS unsigned*)(ws + off), T, RLX_AGENT); }
DI unsigned flag_get(char* ws, unsigned off) { return __hip_atomic_load((GAS unsigned*)(ws + off), RLX_AGENT); }
#define ORDER_BEHIND_POLL() __builtin_amdgcn_fence(__ATOMIC_ACQUIRE, "wavefront")

DI void poll_words(char* ws, unsigned off, bool need, unsigned T) {
  for (unsigned spins = 0; spins < SPIN_CAP; ++spins) {
    unsigned v = flag_get(ws, off);
    if (__all(!need || v == T)) break;
    __builtin_amdgcn_s_sleep(1);
  }
  ORDER_BEHIND_POLL();
}
DI void poll_all_p(char* ws, int lane, unsigned T) {
  rsrc_t rs = mkrs(ws, WS_END);
  for (unsigned spins = 0; spins < SPIN_CAP; ++spins) {
    v4u v = __builtin_amdgcn_raw_buffer_load_b128(rs, WS_PF + lane * 16, 0, SC1);
    asm volatile("" ::: "memory");
    if (__all(v.x == T && v.y == T && v.z == T && v.w == T)) break;
    __builtin_amdgcn_s_sleep(1);
  }
  ORDER_BEHIND_POLL();
}
DI void p_arrive(LAS char* L, char* ws, int b, int lane) {
  VMW(0);
  if (lane == 0) {
    unsigned old = __hip_atomic_fetch_add((LAS unsigned*)(L + L_CTL + 4), 1u, __ATOMIC_RELAXED, __HIP_MEMORY_SCOPE_WORKGROUP);
    if (old == 5u) flag_set(ws, WS_PF + b * 4, *(volatile LAS unsigned*)(L + L_CTL));
  }
}

DI void build_hc(LAS char* hc, int m, v4u lo, v4u hi) {
  unsigned d[8] = {lo.x, lo.y, lo.z, lo.w, hi.x, hi.y, hi.z, hi.w};
  unsigned o[7];
#pragma unroll
  for (int i = 0; i < 7; ++i) o[i] = __builtin_amdgcn_alignbit(d[i + 1], d[i], 16);
#pragma unroll
  for (int c = 0; c < 8; ++c) {
    const int sg = c + 1;
    v4u v;
    if (sg & 1) { const int i = (sg - 1) / 2; v = v4u{o[i], o[i + 1], o[i + 2], o[i + 3]}; }
    else { const int i = sg / 2; v = v4u{d[i], d[i + 1], d[i + 2], d[i + 3]}; }
    *(LAS v4u*)(hc + c * HC_P + m * 16) = v;
  }
}

template <int N0, int N1>
DI void conv_part(LAS char* hc, LAS char* ur, int lane, float Dd, rsrc_t rws, int d) {
  constexpr int NQ = N1;
  const int b = lane & 15, g = lane >> 4;
  LAS char* ab = hc + (b & 7) * HC_P + (g + (b >> 3)) * 16;
  LAS char* bb = ur + 2 * (2032 - 16 * b + 8 * g) - 512 * (NQ - 1);
  f32x4 acc[N1 - N0];
#pragma unroll
  for (int n = 0; n < N1 - N0; ++n) acc[n] = f32x4{0.f, 0.f, 0.f, 0.f};
#pragma unroll
  for (int r = 0; r < 8; ++r) {
    bf16x8 A[NQ], B[NQ];
#pragma unroll
    for (int q = 0; q < NQ; ++q) {
      A[q] = *(LAS bf16x8*)(ab + 512 * q + 64 * r);
      B[NQ - 1 - q] = *(LAS bf16x8*)(bb + 512 * q + 64 * r);
    }
#pragma unroll
    for (int q = 0; q < NQ; ++q)
#pragma unroll
      for (int n = N1 - 1; n >= N0; --n)
        if (q <= n) acc[n - N0] = mfma16(A[q], B[n - q], acc[n - N0]);
  }
#pragma unroll
  for (int n = N0; n < N1; ++n) {
    const int s = 256 * n + 16 * b + 4 * g;
    v2u uu = *(LAS v2u*)(ur + 2 * (2044 - s));
    const float u0 = bfhi(uu.y), u1 = bflo(uu.y), u2 = bfhi(uu.x), u3 = bflo(uu.x);
    const f32x4 y = acc[n - N0];
    const unsigned y01 = pk2(y[0], y[1]), y23 = pk2(y[2], y[3]);
    const unsigned k01 = pk2(u0 * Dd, u1 * Dd), k23 = pk2(u2 * Dd, u3 * Dd);
    v2u o;
    o.x = pk2(bflo(y01) + bflo(k01), bfhi(y01) + bfhi(k01));
    o.y = pk2(bflo(y23) + bflo(k23), bfhi(y23) + bfhi(k23));
    st64(o, rws, WS_ZT + d * 4096 + s * 2, 0);
  }
}

__global__ __launch_bounds__(512, 2) void fused_kernel(const u16* __restrict__ x, const u16* __restrict__ kin, const u16* __restrict__ vin,
                                                       const u16* __restrict__ sk, const u16* __restrict__ hk, const u16* __restrict__ sD,
                                                       const u16* __restrict__ wg, const u16* __restrict__ wu, const u16* __restrict__ wd,
                                                       u16* __restrict__ out, char* ws) {
  __shared__ __attribute__((aligned(16))) char smem[LDS_BYTES];
  LAS char* L = (LAS char*)smem;
  const int tid = threadIdx.x, lane = tid & 63, w = __builtin_amdgcn_readfirstlane(tid >> 6), b = blockIdx.x;
  const int xcd = b & 7, j = b >> 3;
  STAMP(0);
  int mt, ft;
  {
    const int mh = xcd & 1, fq = xcd >> 1;
    if (j < 28) { const int idx = fq * 28 + j; ft = idx >> 3; mt = mh * 8 + (idx & 7); }
    else { ft = 14 + ((j - 28) >> 1); mt = mh * 8 + fq * 2 + (j & 1); }
  }
  const int nblk = ft < 14 ? 6 : (ft == 14 ? 2 : 0);
  const int m2 = xcd * 4 + (j & 3), nt = j >> 2;
  const rsrc_t rws = mkrs(ws, WS_END);
  const int h = lane >> 5;
  const int k0 = w < 7 ? 11 * w : 76, nk = w < 6 ? 11 : 10;
  v4u wdf[11][2];
  unsigned T;

  if (w < 4) {
    {
      const int stile = j, dg = xcd, cp = lane & 31;
      const int s0 = 64 * stile + 16 * w + 8 * h, d = 64 * dg + 2 * cp;
      const rsrc_t rk = mkrs(kin, S * D * 2), rv = mkrs(vin, S * D * 2), rw = mkrs(wd, F * D * 2);
      const unsigned vo = s0 * 1024 + d * 2, vm = s0 == 0 ? OOB : vo - 3072;
      unsigned kk[11], vv[8], wr[8];
#pragma unroll
      for (int i = 0; i < 3; ++i) kk[i] = ld32(rk, vm + i * 1024, 0);
#pragma unroll
      for (int i = 0; i < 8; ++i) { kk[3 + i] = ld32(rk, vo, i * 1024); vv[i] = ld32(rv, vo, i * 1024); }
      const v4u sw = *(const v4u*)(sk + d * 4);
      const int uid = b + 256 * w;
      const bool hasu = w < 3 && uid < 688;
      const int uks = uid >> 3, unt = uid & 7;
      if (hasu) {
#pragma unroll
        for (int jj = 0; jj < 8; ++jj) wr[jj] = ld32(rw, (4 * h) * 1024 + cp * 4, (16 * uks + 8 * (jj >> 2) + (jj & 3)) * 1024 + unt * 128);
      }
      float ra[8], rb[8];
      const float a0 = bflo(sw.x), a1 = bfhi(sw.x), a2 = bflo(sw.y), a3 = bfhi(sw.y);
      const float b0 = bflo(sw.z), b1 = bfhi(sw.z), b2 = bflo(sw.w), b3 = bfhi(sw.w);
#pragma unroll
      for (int i = 0; i < 8; ++i) {
        ra[i] = (bflo(kk[i]) * a3 + bflo(kk[i + 1]) * a2 + bflo(kk[i + 2]) * a1 + bflo(kk[i + 3]) * a0) * bflo(vv[i]);
        rb[i] = (bfhi(kk[i]) * b3 + bfhi(kk[i + 1]) * b2 + bfhi(kk[i + 2]) * b1 + bfhi(kk[i + 3]) * b0) * bfhi(vv[i]);
      }
      const v4u oa = {pk2(ra[7], ra[6]), pk2(ra[5], ra[4]), pk2(ra[3], ra[2]), pk2(ra[1], ra[0])};
      const v4u ob = {pk2(rb[7], rb[6]), pk2(rb[5], rb[4]), pk2(rb[3], rb[2]), pk2(rb[1], rb[0])};
      st128(oa, rws, WS_KVR + d * 4096 + (2040 - s0) * 2, 0);
      st128(ob, rws, WS_KVR + d * 4096 + 4096 + (2040 - s0) * 2, 0);
      if (hasu) {
        v4u ev, od;
#pragma unroll
        for (int jj = 0; jj < 4; ++jj) {
          ev[jj] = __builtin_amdgcn_perm(wr[2 * jj + 1], wr[2 * jj], 0x05040100u);
          od[jj] = __builtin_amdgcn_perm(wr[2 * jj + 1], wr[2 * jj], 0x07060302u);
        }
        st128(ev, rws, lane * 16, WS_WDT + (unt * NFB + uks) * 2048);
        st128(od, rws, lane * 16, WS_WDT + (unt * NFB + uks) * 2048 + 1024);
      }
    }
    STAMP(1);
    const int wm = w & 1, wn = w >> 1;
    f32x16 acc[2][3];
#pragma unroll
    for (int t = 0; t < 2; ++t)
#pragma unroll
      for (int i = 0; i < 3; ++i)
#pragma unroll
        for (int r = 0; r < 16; ++r) acc[t][i][r] = 0.f;
    const int tok = 64 * wm + (lane & 31);
    const unsigned xa0 = tok * 128 + ((h ^ ((tok >> 1) & 7)) << 4);
    unsigned wb[3];
    {
      const int mat = (lane >> 4) & 1, q = (lane & 15) >> 2, p = lane & 3;
#pragma unroll
      for (int i = 0; i < 3; ++i) {
        const int fb = 3 * wn + i;
        wb[i] = L_XW + mat * 12288 + h * 1536 + q * 64 + p * 8 + (fb >> 1) * 256 + (((fb & 1) ^ mat) << 5);
      }
    }
    bf16x8 xf[2][2], wf[2][3];
    auto ldfr = [&](unsigned so, int ks, int buf) {
      LAS char* xp = L + ((xa0 + so) ^ (ks << 5));
      xf[buf][0] = *(LAS bf16x8*)(xp);
      xf[buf][1] = *(LAS bf16x8*)(xp + 4096);
#pragma unroll
      for (int i = 0; i < 3; ++i) {
        LAS char* wp = L + (wb[i] + so) + ks * 3072;
        s16x4 lo = __builtin_amdgcn_ds_read_tr16_b64_v4i16((LAS s16x4*)(wp));
        s16x4 hi = __builtin_amdgcn_ds_read_tr16_b64_v4i16((LAS s16x4*)(wp + 768));
        wf[buf][i] = __builtin_shufflevector(lo, hi, 0, 1, 2, 3, 4, 5, 6, 7);
      }
    };
    auto mm = [&](int buf) {
#pragma unroll
      for (int i = 0; i < 3; ++i)
#pragma unroll
        for (int t = 0; t < 2; ++t) acc[t][i] = mfma32(wf[buf][i], xf[buf][t], acc[t][i]);
    };
    BAR();
    STAMP(2);
    unsigned so = 0;
    ldfr(0, 0, 0);
#pragma unroll 1
    for (int s = 0; s < 8; ++s) {
      ldfr(so, 1, 1); mm(0);
      ldfr(so, 2, 0); mm(1);
      ldfr(so, 3, 1); mm(0);
      if (s == 1) p_arrive(L, ws, b, lane);
      so = so == 2 * SLOT ? 0 : so + SLOT;
      BAR();
      ldfr(so, 0, 0);
      mm(1);
    }
    STAMP(3);
    T = __builtin_amdgcn_readfirstlane(*(volatile LAS unsigned*)(L + L_CTL));
    {
      const int mt64 = 2 * mt + wm;
#pragma unroll
      for (int t = 0; t < 2; ++t) {
        const f32x2 st = *(LAS f32x2*)(L + L_ST + (tok + 32 * t) * 8);
        const float nm = -st[0], r = st[1];
        const float K1 = -2.3022082f * r, K2 = (-2.3022082f * 0.044715f) * r * r * r, iR2 = 1.0f / (r * r);
#pragma unroll
        for (int i = 0; i < 3; ++i) {
          if (3 * wn + i < nblk) {
            LAS char* cp = L + L_CS + (16 * (3 * wn + i) + 4 * h) * 4;
            const f32x4 g0 = *(LAS f32x4*)(cp), g1 = *(LAS f32x4*)(cp + 32), u0 = *(LAS f32x4*)(cp + 384), u1 = *(LAS f32x4*)(cp + 416);
            float hv[8];
#pragma unroll
            for (int e = 0; e < 8; ++e) {
              const float cg = e < 4 ? g0[e & 3] : g1[e & 3], cu = e < 4 ? u0[e & 3] : u1[e & 3];
              const float G = __builtin_fmaf(nm, cg, acc[t][i][e]), U = __builtin_fmaf(nm, cu, acc[t][i][8 + e]);
              const float ww = G * __builtin_fmaf(G * G, K2, K1);
              const float ex = __builtin_amdgcn_exp2f(ww);
              const float rc = __builtin_amdgcn_rcpf(__builtin_fmaf(ex, iR2, iR2));
              hv[e] = (G * U) * rc;
            }
            const v4u o = {pk2(hv[0], hv[1]), pk2(hv[2], hv[3]), pk2(hv[4], hv[5]), pk2(hv[6], hv[7])};
            st128(o, rws, lane * 16, WS_HT + ((mt64 * NFB + ft * 6 + 3 * wn + i) * 2 + t) * 1024);
          }
        }
      }
      STAMP(4);
      VMW(0);
      if (lane == 0 && 3 * wn < nblk) flag_set(ws, WS_HF + (mt64 * 32 + ft * 2 + wn) * 4, T);
    }
    STAMP(5);
#pragma unroll
    for (int i = 0; i < 11; ++i)
#pragma unroll
      for (int e = 0; e < 2; ++e) wdf[i][e] = ld128(rws, i < nk ? lane * 16 : OOB, WS_WDT + ((nt * NFB + k0 + i) * 2 + e) * 1024, 1);
    __builtin_amdgcn_sched_barrier(0);
  } else {
    const int chl = (w - 4) >> 1, d0 = 2 * b;
    if (w < 6) {
      const rsrc_t rx = mkrs(x, nblk ? S * D * 2 : 0), rg = mkrs(wg, D * WROW), ru = mkrs(wu, D * WROW);
      unsigned vx[2], vw[3];
#pragma unroll
      for (int p = 0; p < 2; ++p) vx[p] = (lane >> 3) * 1024 + (((lane & 7) ^ (4 * p + (lane >> 4))) << 4);
#pragma unroll
      for (int n = 0; n < 3; ++n) {
        const int G = 4 * n + (lane >> 4), kq = G / 3, uc = G - 3 * kq;
        vw[n] = 2 * uc < nblk ? (kq * 4 + ((lane >> 2) & 3)) * WROW + uc * 64 + (lane & 3) * 16 : OOB;
      }
      auto stage = [&](int s, unsigned so) {
        LAS char* dst = L + so;
        const unsigned sw = s * 64 * WROW + ft * 192;
        if (w == 4) {
#pragma unroll
          for (int i = 0; i < 16; ++i) dma(rx, dst + i * 1024, vx[i & 1], (mt * 128 + 8 * i) * 1024 + s * 128);
#pragma unroll
          for (int n = 0; n < 4; ++n) dma(rg, dst + L_XW + n * 1024, vw[n % 3], sw + (n / 3) * 16 * WROW);
        } else {
#pragma unroll
          for (int n = 4; n < 12; ++n) dma(rg, dst + L_XW + n * 1024, vw[n % 3], sw + (n / 3) * 16 * WROW);
#pragma unroll
          for (int n = 0; n < 12; ++n) dma(ru, dst + L_UP + n * 1024, vw[n % 3] ^ 32, sw + (n / 3) * 16 * WROW);
        }
      };
      stage(0, 0);
      stage(1, SLOT);
      VMW(20);
      BAR();
      unsigned so = 2 * SLOT;
#pragma unroll 1
      for (int s = 0; s < 6; ++s) {
        stage(s + 2, so);
        so = so == 2 * SLOT ? 0 : so + SLOT;
        VMW(20);
        BAR();
      }
      VMW(0);
      BAR();
    } else {
      const rsrc_t rh0 = mkrs(hk + d0 * S, S * 2), rh1 = mkrs(hk + (d0 + 1) * S, S * 2);
      const int m0 = 1 + (w - 6) * 128 + lane;
      v4u pl[16];
      unsigned E = 0;
      if (w == 6) {
        E = flag_get(ws, WS_EPOCH);
        const rsrc_t rx = mkrs(x, S * D * 2);
#pragma unroll
        for (int i = 0; i < 8; ++i) pl[i] = ld128(rx, (lane >> 3) * 1024 + (lane & 7) * 16, b * 8192 + i * 128, 0);
      } else {
        const int c = lane % 24, rgp = lane / 24;
        const int cc = c < 12 ? c : c - 12;
        const bool ok = lane < 48 && cc < 2 * nblk;
        const char* base = (const char*)(c < 12 ? wg : wu) + (ok ? (size_t)((32 * mt + 16 * rgp) * WROW + ft * 192 + cc * 16) : 0);
#pragma unroll
        for (int i = 0; i < 16; ++i) pl[i] = *(const v4u*)(base + i * WROW);
      }
      v4u hl[2][2];
#pragma unroll
      for (int i = 0; i < 2; ++i) { hl[i][0] = ld128(rh0, (m0 + 64 * i - 2) * 16, 0, 0); hl[i][1] = ld128(rh0, (m0 + 64 * i - 1) * 16, 0, 0); }
      __builtin_amdgcn_sched_barrier(0);
      BAR();
      if (w == 6) {
        float sum = 0.f;
#pragma unroll
        for (int i = 0; i < 8; ++i)
#pragma unroll
          for (int e = 0; e < 4; ++e) sum += bflo(pl[i][e]) + bfhi(pl[i][e]);
        sum += __shfl_xor(sum, 1); sum += __shfl_xor(sum, 2); sum += __shfl_xor(sum, 4);
        const float mean = sum * (1.0f / D);
        float sq = 0.f;
#pragma unroll
        for (int i = 0; i < 8; ++i)
#pragma unroll
          for (int e = 0; e < 4; ++e) { const float a = bflo(pl[i][e]) - mean, c = bfhi(pl[i][e]) - mean; sq = __builtin_fmaf(a, a, sq); sq = __builtin_fmaf(c, c, sq); }
        sq += __shfl_xor(sq, 1); sq += __shfl_xor(sq, 2); sq += __shfl_xor(sq, 4);
        const float sd = rbf(__builtin_sqrtf(rbf(rbf(sq * (1.0f / D)) + 1e-5f)));
        if ((lane & 7) == 0) st64(v2u{__float_as_uint(rbf(mean)), __float_as_uint(1.0f / sd)}, rws, WS_STATS + (b * 8 + (lane >> 3)) * 8, 0);
        if (lane == 0) { *(volatile LAS unsigned*)(L + L_CTL) = E + 1u; *(volatile LAS unsigned*)(L + L_CTL + 4) = 0u; }
      } else {
        float cs[8];
#pragma unroll
        for (int e = 0; e < 8; ++e) cs[e] = 0.f;
#pragma unroll
        for (int i = 0; i < 16; ++i)
#pragma unroll
          for (int e = 0; e < 4; ++e) { cs[2 * e] += bflo(pl[i][e]); cs[2 * e + 1] += bfhi(pl[i][e]); }
#pragma unroll
        for (int e = 0; e < 8; ++e) cs[e] += __shfl_down(cs[e], 24);
        const unsigned vo = (lane < 24 && nblk) ? WS_CSP + ((ft * 16 + mt) * 192 + lane * 8) * 4 : OOB;
        st128(v4u{__float_as_uint(cs[0]), __float_as_uint(cs[1]), __float_as_uint(cs[2]), __float_as_uint(cs[3])}, rws, vo, 0);
        st128(v4u{__float_as_uint(cs[4]), __float_as_uint(cs[5]), __float_as_uint(cs[6]), __float_as_uint(cs[7])}, rws, vo + 16, 0);
      }
      BAR();
      p_arrive(L, ws, b, lane);
      build_hc(L + L_HC0, m0, hl[0][0], hl[0][1]);
      build_hc(L + L_HC0, m0 + 64, hl[1][0], hl[1][1]);
      if (w == 6) {
        if (lane < 8) *(LAS v4u*)(L + L_HC0 + lane * HC_P) = v4u{0, 0, 0, 0};
        else if (lane < 40) *(LAS v4u*)(L + L_UR0 + 4096 + (lane - 8) * 16) = v4u{0, 0, 0, 0};
      }
#pragma unroll
      for (int i = 0; i < 2; ++i) { hl[i][0] = ld128(rh1, (m0 + 64 * i - 2) * 16, 0, 0); hl[i][1] = ld128(rh1, (m0 + 64 * i - 1) * 16, 0, 0); }
      __builtin_amdgcn_sched_barrier(0);
      BAR();
      BAR();
      BAR();
      const unsigned Tl = __builtin_amdgcn_readfirstlane(*(volatile LAS unsigned*)(L + L_CTL));
      poll_all_p(ws, lane, Tl);
      if (b == 0 && w == 6 && lane == 0) flag_set(ws, WS_EPOCH, Tl);
      v4u ul[4];
#pragma unroll
      for (int i = 0; i < 4; ++i) ul[i] = ld128(rws, lane * 16, WS_KVR + (d0 + (w - 6)) * 4096 + i * 1024, 1);
      if (w == 6) {
        pl[0] = ld128(rws, lane * 16, WS_STATS + mt * 1024, 1);
      } else {
#pragma unroll
        for (int i = 0; i < 16; ++i) pl[i] = ld128(rws, (lane < 48 && nblk) ? lane * 16 : OOB, WS_CSP + (ft * 16 + i) * 768, 1);
      }
      __builtin_amdgcn_sched_barrier(0);
      BAR();
      if (w == 6) {
#pragma unroll
        for (int i = 0; i < 4; ++i) *(LAS v4u*)(L + L_UR0 + i * 1024 + lane * 16) = ul[i];
        *(LAS v4u*)(L + L_ST + lane * 16) = pl[0];
      } else {
        f32x4 c = {0.f, 0.f, 0.f, 0.f};
#pragma unroll
        for (int i = 0; i < 16; ++i) c += __builtin_bit_cast(f32x4, pl[i]);
        if (lane < 48) *(LAS f32x4*)(L + L_CS + lane * 16) = c;
      }
      BAR();
      build_hc(L + L_HC1, m0, hl[0][0], hl[0][1]);
      build_hc(L + L_HC1, m0 + 64, hl[1][0], hl[1][1]);
      if (w == 6) {
        if (lane < 8) *(LAS v4u*)(L + L_HC1 + lane * HC_P) = v4u{0, 0, 0, 0};
        else if (lane < 40) *(LAS v4u*)(L + L_UR1 + 4096 + (lane - 8) * 16) = v4u{0, 0, 0, 0};
      } else {
#pragma unroll
        for (int i = 0; i < 4; ++i) *(LAS v4u*)(L + L_UR1 + i * 1024 + lane * 16) = ul[i];
      }
      BAR();
    }
    STAMP(2);
#pragma unroll
    for (int i = 0; i < 11; ++i)
#pragma unroll
      for (int e = 0; e < 2; ++e) wdf[i][e] = ld128(rws, i < nk ? lane * 16 : OOB, WS_WDT + ((nt * NFB + k0 + i) * 2 + e) * 1024, 1);
    const int d = d0 + chl;
    const float Dd = bflo(sD[d]);
    __builtin_amdgcn_sched_barrier(0);
    BAR();
    STAMP(3);
    T = __builtin_amdgcn_readfirstlane(*(volatile LAS unsigned*)(L + L_CTL));
    LAS char* hc = L + (chl ? L_HC1 : L_HC0);
    LAS char* ur = L + (chl ? L_UR1 : L_UR0);
    if (w & 1) conv_part<6, 8>(hc, ur, lane, Dd, rws, d);
    else conv_part<0, 6>(hc, ur, lane, Dd, rws, d);
    STAMP(4);
    VMW(0);
    if (lane == 0) flag_set(ws, WS_ZF + (d * 2 + (w & 1)) * 4, T);
    STAMP(5);
  }

  {
    const int tb = w >> 2, q = w & 3;
    const int tok = 64 * m2 + 32 * tb + (lane & 31), dd = 64 * nt + 16 * q + 8 * h;
    const v4u xv = *(const v4u*)(x + tok * D + dd);
    const v2u stv2 = __builtin_amdgcn_raw_buffer_load_b64(rws, WS_STATS + tok * 8, 0, SC1);
    poll_words(ws, WS_HF + (m2 * 32 + (lane & 31)) * 4, lane < 32 && lane >= k0 / 3 && lane <= (k0 + nk - 1) / 3, T);
    STAMP(6);
    v4u hf[11][2];
#pragma unroll
    for (int i = 0; i < 11; ++i)
#pragma unroll
      for (int t = 0; t < 2; ++t) hf[i][t] = ld128(rws, i < nk ? lane * 16 : OOB, WS_HT + ((m2 * NFB + k0 + i) * 2 + t) * 1024, 1);
    __builtin_amdgcn_sched_barrier(0);
    f32x16 acc[2][2];
#pragma unroll
    for (int t = 0; t < 2; ++t)
#pragma unroll
      for (int e = 0; e < 2; ++e)
#pragma unroll
        for (int r = 0; r < 16; ++r) acc[t][e][r] = 0.f;
#pragma unroll
    for (int i = 0; i < 11; ++i)
#pragma unroll
      for (int t = 0; t < 2; ++t)
#pragma unroll
        for (int e = 0; e < 2; ++e) acc[t][e] = mfma32(asb(wdf[i][e]), asb(hf[i][t]), acc[t][e]);
    STAMP(7);
    poll_words(ws, WS_ZF + ((64 * nt + 8 * w) * 2 + (lane & 15)) * 4, lane < 16, T);
    const v4u zv = ld128(rws, WS_ZT + (64 * nt + 8 * w + (lane >> 3)) * 4096 + (64 * m2 + 8 * (lane & 7)) * 2, 0, 1);
#pragma unroll
    for (int t = 0; t < 2; ++t)
#pragma unroll
      for (int qq = 0; qq < 4; ++qq) {
        v4u o;
#pragma unroll
        for (int r = 0; r < 4; ++r) o[r] = pk2(acc[t][0][4 * qq + r], acc[t][1][4 * qq + r]);
        *(LAS v4u*)(L + L_RED + (w * 8 + t * 4 + qq) * 1024 + lane * 16) = o;
      }
    *(LAS v4u*)(L + L_ZT + (8 * w + (lane >> 3)) * ZT_P + (lane & 7) * 16) = zv;
    BAR();
    STAMP(8);
    float f[8];
#pragma unroll
    for (int e = 0; e < 8; ++e) f[e] = 0.f;
#pragma unroll
    for (int ww = 0; ww < 8; ++ww) {
      const v4u p = *(LAS v4u*)(L + L_RED + (ww * 8 + w) * 1024 + lane * 16);
#pragma unroll
      for (int e = 0; e < 4; ++e) { f[2 * e] += bflo(p[e]); f[2 * e + 1] += bfhi(p[e]); }
    }
    LAS char* zp = L + L_ZT + (16 * q + 8 * h + ((lane & 15) >> 2)) * ZT_P + (32 * tb + 16 * ((lane >> 4) & 1) + 4 * (lane & 3)) * 2;
    const v2u z0 = __builtin_bit_cast(v2u, __builtin_amdgcn_ds_read_tr16_b64_v4i16((LAS s16x4*)(zp)));
    const v2u z1 = __builtin_bit_cast(v2u, __builtin_amdgcn_ds_read_tr16_b64_v4i16((LAS s16x4*)(zp + 4 * ZT_P)));
    const unsigned zz[4] = {z0.x, z0.y, z1.x, z1.y};
    const float m = __uint_as_float(stv2.x), r = __uint_as_float(stv2.y);
    v4u o;
#pragma unroll
    for (int e = 0; e < 4; ++e) {
      const float x0 = bflo(xv[e]), x1 = bfhi(xv[e]);
      const unsigned c = pk2(x0 - m, x1 - m);
      const unsigned xn = pk2(bflo(c) * r, bfhi(c) * r);
      const unsigned a = pk2(bflo(xn) * bflo(zz[e]), bfhi(xn) * bfhi(zz[e]));
      const unsigned s1 = pk2(x0 + bflo(a), x1 + bfhi(a));
      const unsigned ff = pk2(f[2 * e], f[2 * e + 1]);
      o[e] = pk2(bflo(s1) + bflo(ff), bfhi(s1) + bfhi(ff));
    }
    *(v4u*)(out + tok * D + dd) = o;
    STAMP(9);
  }
}

extern "C" void kernel_launch(void* const* d_in, const int* in_sizes, int n_in, void* d_out, int out_size, void* d_ws, size_t ws_size,
                              hipStream_t stream) {
  fused_kernel<<<256, 512, 0, stream>>>((const u16*)d_in[0], (const u16*)d_in[1], (const u16*)d_in[2], (const u16*)d_in[3], (const u16*)d_in[4],
                                        (const u16*)d_in[5], (const u16*)d_in[7], (const u16*)d_in[8], (const u16*)d_in[9], (u16*)d_out,
                                        (char*)d_ws);
}
```

```cpp
#include <hip/hip_runtime.h>
#include <hip/hip_bf16.h>
#include <stdint.h>

typedef unsigned short u16;
typedef unsigned v4u __attribute__((ext_vector_type(4)));
typedef unsigned v2u __attribute__((ext_vector_type(2)));
typedef short bf16x8 __attribute__((ext_vector_type(8)));
typedef short s16x4 __attribute__((ext_vector_type(4)));
typedef float f32x16 __attribute__((ext_vector_type(16)));
typedef float f32x4 __attribute__((ext_vector_type(4)));
typedef float f32x2 __attribute__((ext_vector_type(2)));
typedef __bf16 bf2 __attribute__((ext_vector_type(2)));
#define LAS __attribute__((address_space(3)))
#define GAS __attribute__((address_space(1)))
#define DI __device__ __forceinline__
#define AI __attribute__((always_inline))
typedef __amdgpu_buffer_rsrc_t rsrc_t;

constexpr int S = 2048, D = 512, F = 1376, NFB = 86;
constexpr int WROW = F * 2;
constexpr unsigned FL = 128;
constexpr unsigned WS_EPOCH = 0, WS_PF = 1024, WS_KF = WS_PF + 256 * FL, WS_ZF = WS_KF + 256 * FL, WS_HF = WS_ZF + 256 * FL;
constexpr unsigned WS_STATS = WS_HF + 1024 * FL;
constexpr unsigned WS_CSP = WS_STATS + 16384;
constexpr unsigned WS_KVR = 524288;
static_assert(WS_CSP + 15 * 16 * 768 <= WS_KVR, "ws");
constexpr unsigned WS_ZT = WS_KVR + 2097152;
constexpr unsigned WS_WDT = WS_ZT + 2097152;
constexpr unsigned WS_HT = WS_WDT + 8 * 86 * 2048;
constexpr unsigned WS_END = WS_HT + 32 * 86 * 2048;
constexpr unsigned WS_STAMP = WS_END;
constexpr int SLOT = 40960, L_XW = 16384, L_UP = 28672;
constexpr int L_CS = 122880, L_ST = L_CS + 768, L_CTL = L_CS + 1792;
constexpr int L_ZCT = L_CTL + 4, L_CVK = L_CTL + 8, L_EPI = L_CTL + 12, L_FREE = L_CTL + 16, L_FULL = L_CTL + 32, L_POK = L_CTL + 64, L_ISS = L_CTL + 88,
              L_STG = L_CTL + 72, L_PCT = L_CTL + 76, L_KCT = L_CTL + 80, L_ORD = L_CTL + 84, L_KVD = L_CTL + 96;
constexpr int HC_P = 4160, HC_B = 8 * HC_P;
constexpr int L_HC0 = 124928, L_UR0 = L_HC0 + HC_B, L_HC1 = 2 * SLOT, L_UR1 = L_HC1 + HC_B;
constexpr int L_KS = L_HC0, L_VS = L_KS + 9216, L_WS = L_VS + 8192, L_KO = L_WS + 6144;
constexpr int L_RED = 0, L_TP = 65536, L_ZS = 73728;
constexpr int LDS_BYTES = 163840;
static_assert(L_UR0 + 4608 <= LDS_BYTES && L_UR1 + 4608 <= 3 * SLOT, "lds");
#define NEPI 2
#define L1SLEEP 10
#define EARLYWD 1
#define PSLEEP 4
#define HSLEEP 20
#define PFSLEEP 20
#define POLLST 5
#define CONVGO 9
#define CODEPF 0
#define DATAPF 0
#define SCALAREPI 0
#define CSST 6
#define KVST 1
#define HWAIT 1
#define HPRIO 3
constexpr int SC1 = 16;
constexpr unsigned OOB = 0x80000000u;
constexpr unsigned SPIN_CAP = 1u << 14;

#define BAR() asm volatile("s_waitcnt lgkmcnt(0)\n\ts_barrier" ::: "memory")
#define BARS(i) do { STAMP(10 + (i)); BAR(); } while (0)
#define SGB(m, n) __builtin_amdgcn_sched_group_barrier(m, n, 0)
#define SCHED() __builtin_amdgcn_sched_barrier(0)
#define VMW(n) asm volatile("s_waitcnt vmcnt(%0)" ::"i"(n) : "memory")
#define RLX_AGENT __ATOMIC_RELAXED, __HIP_MEMORY_SCOPE_AGENT

#define STAMPCLK() __builtin_amdgcn_s_memrealtime()
#define STAMP(i)

DI float u2f(unsigned u) { return __builtin_bit_cast(float, u); }
DI unsigned f2u(float f) { return __builtin_bit_cast(unsigned, f); }
DI bool all_of(bool c) { return __builtin_amdgcn_ballot_w64(!c) == 0; }
template <int CTRL> DI float dpp(float v) { return u2f(__builtin_amdgcn_update_dpp(0u, f2u(v), CTRL, 0xf, 0xf, true)); }
DI float sum8(float v) { v += dpp<0xB1>(v); v += dpp<0x4E>(v); v += dpp<0x141>(v); return v; }
DI unsigned umin(unsigned a, unsigned b) { return a < b ? a : b; }
DI float sadd(float a, float b) { float d; asm("v_add_f32 %0, %1, %2" : "=v"(d) : "v"(a), "v"(b)); return d; }
DI float ssub(float a, float b) { float d; asm("v_sub_f32 %0, %1, %2" : "=v"(d) : "v"(a), "v"(b)); return d; }
DI float smul(float a, float b) { float d; asm("v_mul_f32 %0, %1, %2" : "=v"(d) : "v"(a), "v"(b)); return d; }
DI float sfma(float a, float b, float c) { float d; asm("v_fma_f32 %0, %1, %2, %3" : "=v"(d) : "v"(a), "v"(b), "v"(c)); return d; }
DI float bflo(unsigned u) { return u2f(u << 16); }
DI float bfhi(unsigned u) { return u2f(u & 0xffff0000u); }
DI unsigned pk2(float a, float b) { f32x2 v = {a, b}; return __builtin_bit_cast(unsigned, __builtin_convertvector(v, bf2)); }
DI float rbf(float a) { return bflo(pk2(a, a)); }
DI rsrc_t mkrs(const void* p, unsigned n) { return __builtin_amdgcn_make_buffer_rsrc((void*)p, 0, n, 0x00020000); }
DI v4u ld128(rsrc_t r, unsigned vo, unsigned so, int aux) { return aux ? __builtin_amdgcn_raw_buffer_load_b128(r, vo, so, SC1) : __builtin_amdgcn_raw_buffer_load_b128(r, vo, so, 0); }
DI unsigned ld32(rsrc_t r, unsigned vo, unsigned so) { return __builtin_amdgcn_raw_buffer_load_b32(r, vo, so, 0); }
DI void st128(v4u v, rsrc_t r, unsigned vo, unsigned so) { __builtin_amdgcn_raw_buffer_store_b128(v, r, vo + so, 0, SC1); }
DI void st64(v2u v, rsrc_t r, unsigned vo, unsigned so) { __builtin_amdgcn_raw_buffer_store_b64(v, r, vo, so, SC1); }
DI void dma(rsrc_t r, LAS char* dst, unsigned vo, unsigned so) { __builtin_amdgcn_raw_ptr_buffer_load_lds(r, (LAS void*)dst, 16, vo, so, 0, 0); }
DI bf16x8 asb(v4u v) { return __builtin_bit_cast(bf16x8, v); }
DI f32x16 mfma32(bf16x8 a, bf16x8 b, f32x16 c) { return __builtin_amdgcn_mfma_f32_32x32x16_bf16(a, b, c, 0, 0, 0); }
DI f32x4 mfma16(bf16x8 a, bf16x8 b, f32x4 c) { return __builtin_amdgcn_mfma_f32_16x16x32_bf16(a, b, c, 0, 0, 0); }
DI void word_set(char* ws, unsigned off, unsigned T) { __hip_atomic_store((GAS unsigned*)(ws + off), T, RLX_AGENT); }
DI void flag_set(rsrc_t rws, unsigned off, unsigned T, int lane, bool on = true) { st128(v4u{T, T, T, T}, rws, (lane < 8 && on) ? lane * 16 : OOB, off); }
DI unsigned flag_get(char* ws, unsigned off) { return __hip_atomic_load((GAS unsigned*)(ws + off), RLX_AGENT); }
#define ORDER_BEHIND_POLL() __builtin_amdgcn_fence(__ATOMIC_ACQUIRE, "wavefront")

template <int SLEEP = 3>
DI void poll_words(char* ws, unsigned off, unsigned T) {
#pragma nounroll
  for (unsigned spins = 0; spins < SPIN_CAP; ++spins) {
    const unsigned v = flag_get(ws, off);
    if (all_of(v == T)) break;
    __builtin_amdgcn_s_sleep(SLEEP);
  }
  ORDER_BEHIND_POLL();
}
template <int SLEEP>
DI void poll_deep(char* ws, unsigned off, unsigned T, unsigned v0) {
#pragma nounroll
  for (unsigned spins = 0; spins < SPIN_CAP; ++spins) {
    const unsigned v1 = flag_get(ws, off);
    if (all_of(v0 == T)) break;
    __builtin_amdgcn_s_sleep(SLEEP);
    v0 = flag_get(ws, off);
    if (all_of(v1 == T)) break;
    __builtin_amdgcn_s_sleep(SLEEP);
  }
  ORDER_BEHIND_POLL();
}
DI void poll_words2(char* ws, unsigned off0, unsigned off1, unsigned T) {
#pragma nounroll
  for (unsigned spins = 0; spins < SPIN_CAP; ++spins) {
    const unsigned v0 = flag_get(ws, off0), v1 = flag_get(ws, off1);
    if (all_of(v0 == T && v1 == T)) break;
    __builtin_amdgcn_s_sleep(6);
  }
  ORDER_BEHIND_POLL();
}
DI int wg_of_tile(int mt, int ft) {
  const int mh = mt >> 3, ml = mt & 7, idx = ft * 8 + ml;
  const int fq = ft < 14 ? idx / 28 : ml >> 1;
  const int j = ft < 14 ? idx - 28 * fq : 28 + 2 * (ft - 14) + (ml & 1);
  return j * 8 + fq * 2 + mh;
}
DI unsigned lds_get(LAS char* p) { return __builtin_amdgcn_readfirstlane(*(volatile LAS unsigned*)p); }
DI void lds_set(LAS char* p, unsigned v) { *(volatile LAS unsigned*)p = v; }
DI void wait_word(LAS char* p, unsigned want) {
#pragma nounroll
  for (unsigned spins = 0; spins < (1u << 18) && lds_get(p) < want; ++spins) __builtin_amdgcn_s_sleep(1);
  asm volatile("" ::: "memory");
}
DI void wait_free(LAS char* L, unsigned want) {
#pragma nounroll
  for (unsigned spins = 0; spins < (1u << 18); ++spins) {
    const v4u f = *(volatile LAS v4u*)(L + L_FREE);
    const unsigned m = __builtin_amdgcn_readfirstlane(umin(umin(f.x, f.y), umin(f.z, f.w)));
    if (m >= want) break;
    __builtin_amdgcn_s_sleep(1);
  }
  asm volatile("" ::: "memory");
}
DI void lds_add(LAS char* p, int lane) {
  if (lane == 0) __hip_atomic_fetch_add((LAS unsigned*)p, 1u, __ATOMIC_RELAXED, __HIP_MEMORY_SCOPE_WORKGROUP);
}
DI void arrive(LAS char* L, int ctr, unsigned n, rsrc_t rws, unsigned flag, unsigned T, int lane) {
  VMW(0);
  unsigned old = 0;
  if (lane == 0) old = __hip_atomic_fetch_add((LAS unsigned*)(L + ctr), 1u, __ATOMIC_RELAXED, __HIP_MEMORY_SCOPE_WORKGROUP);
  if (__builtin_amdgcn_readfirstlane(old) == n - 1) flag_set(rws, flag, T, lane);
}

DI void build_hc(LAS char* hc, int m, v4u lo, v4u hi) {
  unsigned d[8] = {lo.x, lo.y, lo.z, lo.w, hi.x, hi.y, hi.z, hi.w};
  unsigned o[7];
#pragma unroll
  for (int i = 0; i < 7; ++i) o[i] = __builtin_amdgcn_alignbit(d[i + 1], d[i], 16);
#pragma unroll
  for (int c = 0; c < 8; ++c) {
    const int sg = c + 1;
    v4u v;
    if (sg & 1) { const int i = (sg - 1) / 2; v = v4u{o[i], o[i + 1], o[i + 2], o[i + 3]}; }
    else { const int i = sg / 2; v = v4u{d[i], d[i + 1], d[i + 2], d[i + 3]}; }
    *(LAS v4u*)(hc + c * HC_P + m * 16) = v;
  }
}

template <int N0, int N1>
DI void conv_part(LAS char* hc, LAS char* ur, int lane, float Dd, rsrc_t rws, int d) {
  constexpr int NQ = N1;
  const int b = lane & 15, g = lane >> 4;
  LAS char* ab = hc + (b & 7) * HC_P + (g + (b >> 3)) * 16;
  LAS char* bb = ur + 2 * (2032 - 16 * b + 8 * g) - 512 * (NQ - 1);
  f32x4 acc[N1 - N0];
#pragma unroll
  for (int n = 0; n < N1 - N0; ++n) acc[n] = f32x4{0.f, 0.f, 0.f, 0.f};
  bf16x8 A[2][NQ], B[2][NQ];
#pragma unroll
  for (int q = 0; q < NQ; ++q) {
    A[0][q] = *(LAS bf16x8*)(ab + 512 * q);
    B[0][N0 ? NQ - 1 - q : q] = *(LAS bf16x8*)(bb + 512 * (N0 ? q : NQ - 1 - q));
  }
  SCHED();
#pragma unroll
  for (int r = 0; r < 8; ++r) {
    const int c = r & 1;
    if (r < 7) {
#pragma unroll
      for (int q = 0; q < NQ; ++q) {
        A[c ^ 1][q] = *(LAS bf16x8*)(ab + 512 * q + 64 * (r + 1));
        B[c ^ 1][N0 ? NQ - 1 - q : q] = *(LAS bf16x8*)(bb + 512 * (N0 ? q : NQ - 1 - q) + 64 * (r + 1));
      }
    }
    if (N0) {
#pragma unroll
      for (int q = 0; q < NQ; ++q) {
        acc[1] = mfma16(A[c][q], B[c][7 - q], acc[1]);
        if (q) acc[0] = mfma16(A[c][q - 1], B[c][7 - q], acc[0]);
      }
#pragma unroll
      for (int g2 = 0; g2 < 15; ++g2) { SGB(0x100, 1); SGB(8, 1); }
      SGB(0x100, 1);
    } else {
#pragma unroll
      for (int i = 0; i < NQ; ++i) {
#pragma unroll
        for (int p = 0; p < i; ++p)
          if (i + p < NQ) {
            acc[(i + p) % (N1 - N0)] = mfma16(A[c][i], B[c][p], acc[(i + p) % (N1 - N0)]);
            acc[(i + p) % (N1 - N0)] = mfma16(A[c][p], B[c][i], acc[(i + p) % (N1 - N0)]);
          }
        if (2 * i < NQ) acc[(2 * i) % (N1 - N0)] = mfma16(A[c][i], B[c][i], acc[(2 * i) % (N1 - N0)]);
      }
#pragma unroll
      for (int g2 = 0; g2 < 12; ++g2) { SGB(8, 1); SGB(0x100, 1); }
      SGB(8, 9);
    }
    SCHED();
  }
#pragma unroll
  for (int n = N0; n < N1; ++n) {
    const int s = 256 * n + 16 * b + 4 * g;
    v2u uu = *(LAS v2u*)(ur + 2 * (2044 - s));
    const f32x4 y = acc[n - N0];
    v2u o;
    o.x = pk2(sfma(bfhi(uu.y), Dd, y[0]), sfma(bflo(uu.y), Dd, y[1]));
    o.y = pk2(sfma(bfhi(uu.x), Dd, y[2]), sfma(bflo(uu.x), Dd, y[3]));
    st64(o, rws, WS_ZT + d * 4096 + s * 2, 0);
  }
}

__global__ __launch_bounds__(512, 2) void fused_kernel(const u16* __restrict__ x, const u16* __restrict__ wg, const u16* __restrict__ wu,
                                                            char* ws, const u16* __restrict__ kin, const u16* __restrict__ vin,
                                                            const u16* __restrict__ sk, const u16* __restrict__ hk, const u16* __restrict__ sD,
                                                            const u16* __restrict__ wd, u16* __restrict__ out) {
  __shared__ __attribute__((aligned(16))) char smem[LDS_BYTES];
  LAS char* L = (LAS char*)smem;
  const int tid = threadIdx.x, lane = tid & 63, w = __builtin_amdgcn_readfirstlane(tid >> 6), b = blockIdx.x;
  const int xcd = b & 7, j = b >> 3;
  STAMP(0);
  const int idx = (xcd >> 1) * 28 + j;
  const int ft = j < 28 ? idx >> 3 : 14 + ((j - 28) >> 1);
  const int mt = (xcd & 1) * 8 + (j < 28 ? idx & 7 : (xcd & 6) + (j & 1));
  const int nblk = ft < 14 ? 6 : (ft == 14 ? 2 : 0);
  unsigned vx[2], vw[3];
#pragma unroll
  for (int p = 0; p < 2; ++p) vx[p] = (lane >> 3) * 1024 + (((lane & 7) ^ (4 * p + (lane >> 4))) << 4);
#pragma unroll
  for (int n = 0; n < 3; ++n) {
    const int G = 4 * n + (lane >> 4), kq = (G * 86) >> 8, uc = G - 3 * kq;
    const unsigned v = (kq * 4 + ((lane >> 2) & 3)) * WROW + uc * 64 + (lane & 3) * 16;
    vw[n] = 2 * uc < nblk ? v : OOB;
  }
  asm volatile("" ::"s"(x), "s"(wg), "s"(wu), "s"(ws), "s"(kin), "s"(vin), "s"(sk), "s"(hk), "s"(sD), "s"(wd), "s"(out));
  const rsrc_t rxt = mkrs(x, nblk ? S * D * 2 : 0), rg = mkrs(wg, D * WROW), ru = mkrs(wu, D * WROW);
  auto stage = [&](int s) AI {
    LAS char* dst = L + (s % 3) * SLOT;
    const unsigned sw = s * 64 * WROW + ft * 192;
#pragma unroll
    for (int i = 0; i < 16; ++i) dma(rxt, dst + i * 1024, vx[i & 1], (mt * 128 + 8 * i) * 1024 + s * 128);
#pragma unroll
    for (int n = 0; n < 12; ++n) dma(rg, dst + L_XW + n * 1024, vw[n % 3], sw + (n / 3) * 16 * WROW);
#pragma unroll
    for (int n = 0; n < 12; ++n) dma(ru, dst + L_UP + n * 1024, vw[n % 3] ^ 32, sw + (n / 3) * 16 * WROW);
  };
  if (w == 0 && lane < 28) lds_set(L + L_CTL + 4 + 4 * lane, 0u);
  BAR();
  STAMP(1);
  v4u skv;
  if (w < 4) {
    const rsrc_t rk = mkrs(kin, S * D * 2), rv = mkrs(vin, S * D * 2);
    const int r8 = lane >> 3, p8 = lane & 7;
#pragma unroll
    for (int q = 0; q < 2; ++q) {
      const int i = 2 * w + q, row = 64 * j - 3 + 8 * i + r8;
      dma(rk, L + L_KS + i * 1024, row >= 0 ? row * 1024 + 128 * xcd + ((p8 ^ i) << 4) : OOB, 0);
      dma(rv, L + L_VS + i * 1024, r8 * 1024 + ((p8 ^ i) << 4), (64 * j + 8 * i) * 1024 + 128 * xcd);
    }
    if (w == 0) dma(rk, L + L_KS + 8192, r8 < 3 ? (64 * j + 61 + r8) * 1024 + 128 * xcd + (p8 << 4) : OOB, 0);
    skv = *(const v4u*)(sk + (64 * xcd + 16 * w + 2 * r8) * 4);
  }
  if (w == 4) { stage(0); lds_set(L + L_ORD, 1u); }
  const int m2 = (xcd & 1) * 16 + (xcd >> 1) * 4 + (j & 3), nt = j >> 2;
  const rsrc_t rws = mkrs(ws, WS_END);
  const int h = lane >> 5;
  const int k0 = w < 7 ? 11 * w : 76, nk = w < 6 ? 11 : 10;
  v4u wdf[11][2];
  unsigned T;

  if (w < 4) {
    const int wm = w & 1, wn = w >> 1;
    f32x16 acc[2][3];
#pragma unroll
    for (int t = 0; t < 2; ++t)
#pragma unroll
      for (int i = 0; i < 3; ++i)
#pragma unroll
        for (int r = 0; r < 16; ++r) acc[t][i][r] = 0.f;
#pragma unroll
    for (int t = 0; t < 2; ++t)
#pragma unroll
      for (int i = 0; i < 3; ++i) asm volatile("" : "+v"(acc[t][i]));
    const int tok = 64 * wm + (lane & 31);
    const unsigned xa0 = tok * 128 + ((h ^ ((tok >> 1) & 7)) << 4);
    unsigned wb[3];
    {
      const int mat = (lane >> 4) & 1, q = (lane & 15) >> 2, p = lane & 3;
#pragma unroll
      for (int i = 0; i < 3; ++i) {
        const int fb = 3 * wn + i;
        wb[i] = L_XW + mat * 12288 + h * 1536 + q * 64 + p * 8 + (fb >> 1) * 256 + (((fb & 1) ^ mat) << 5);
      }
    }
    bf16x8 xf[2][2], wf[2][3];
    auto rdw = [&](unsigned so, int ks, int i) AI -> bf16x8 {
      LAS char* wp = L + (wb[i] + so) + ks * 3072;
      s16x4 lo = __builtin_amdgcn_ds_read_tr16_b64_v4i16((LAS s16x4*)(wp));
      s16x4 hi = __builtin_amdgcn_ds_read_tr16_b64_v4i16((LAS s16x4*)(wp + 768));
      return __builtin_shufflevector(lo, hi, 0, 1, 2, 3, 4, 5, 6, 7);
    };
    auto ldfr = [&](unsigned so, int ks, int buf) AI {
      LAS char* xp = L + ((xa0 + so) ^ (ks << 5));
      xf[buf][0] = *(LAS bf16x8*)(xp);
      wf[buf][0] = rdw(so, ks, 0);
      xf[buf][1] = *(LAS bf16x8*)(xp + 4096);
      wf[buf][1] = rdw(so, ks, 1);
      wf[buf][2] = rdw(so, ks, 2);
    };
    auto mm = [&](int buf, int lo, int hi) AI {
#pragma unroll
      for (int e = lo; e < hi; ++e) acc[e & 1][e >> 1] = mfma32(wf[buf][e >> 1], xf[buf][e & 1], acc[e & 1][e >> 1]);
    };
    {
      const int c = lane >> 3, sg = lane & 7;
      VMW(0);
      lds_add(L + L_STG, lane);
      STAMP(10);
      wait_word(L + L_STG, 4);
      unsigned kl[11], vl[8];
      LAS char* kp = L + L_KS + sg * 1024 + (c & 3) * 4;
      const int ch = 2 * w + (c >> 2);
#pragma unroll
      for (int i = 0; i < 11; ++i) kl[i] = *(LAS unsigned*)(kp + i * 128 + ((ch ^ ((sg + (i >> 3)) & 7)) << 4));
#pragma unroll
      for (int i = 0; i < 8; ++i) vl[i] = *(LAS unsigned*)(kp + 9216 + i * 128 + ((ch ^ sg) << 4));
#pragma unroll
      for (int e = 0; e < 2; ++e) {
        const float w0 = bflo(skv[2 * e]), w1 = bfhi(skv[2 * e]), w2 = bflo(skv[2 * e + 1]), w3 = bfhi(skv[2 * e + 1]);
        float kf[11], r[8];
#pragma unroll
        for (int i = 0; i < 11; ++i) kf[i] = e ? bfhi(kl[i]) : bflo(kl[i]);
#pragma unroll
        for (int i = 0; i < 8; ++i)
          r[i] = __builtin_fmaf(kf[i + 3], w0, __builtin_fmaf(kf[i + 2], w1, __builtin_fmaf(kf[i + 1], w2, kf[i] * w3))) * (e ? bfhi(vl[i]) : bflo(vl[i]));
        *(LAS v4u*)(L + L_KO + (16 * w + 2 * c + e) * 128 + (7 - sg) * 16) = v4u{pk2(r[7], r[6]), pk2(r[5], r[4]), pk2(r[3], r[2]), pk2(r[1], r[0])};
      }
      lds_add(L + L_KVD, lane);
    }
    STAMP(19);
    __builtin_amdgcn_s_setprio(2);
    if (nblk == 0) lds_set(L + L_FREE + 4 * w, 8u);
    wait_word(L + L_FULL, 1);
    STAMP(2);
    unsigned so = 0;
    ldfr(0, 0, 0);
#define STEP(ks, nb, cb) ldfr(so, ks, nb); mm(cb, 0, 6); SGB(8, 1); SGB(0x100, 3); SGB(8, 1); SGB(0x100, 1); SGB(8, 1); SGB(0x100, 2); SGB(8, 1); SGB(0x100, 2); SGB(8, 2); SCHED();
#pragma unroll 1
    for (int s = nblk ? 0 : 8; s < 8; ++s) {
      SCHED();
      STEP(1, 1, 0) STEP(2, 0, 1)
      const unsigned fl = *(volatile LAS unsigned*)(L + (s < 7 ? L_FULL + 4 * (s + 1) : L_EPI));
      SCHED();
      STEP(3, 1, 0)
      lds_set(L + L_FREE + 4 * w, s + 1);
      so = so == 2 * SLOT ? 0 : so + SLOT;
      mm(1, 0, 1);
      SCHED();
      STAMP(11 + s);
      if (s < 7 && __builtin_amdgcn_readfirstlane(fl) == 0) wait_word(L + L_FULL + 4 * (s + 1), 1);
      asm volatile("" ::: "memory");
      ldfr(so, 0, 0);
      mm(1, 1, 6);
      SGB(0x100, 3); SGB(8, 1); SGB(0x100, 1); SGB(8, 1); SGB(0x100, 2); SGB(8, 1); SGB(0x100, 2); SGB(8, 2);
      SCHED();
    }
    wait_word(L + L_EPI, 1);
    wait_word(L + L_POK, 2);
    STAMP(3);
    T = lds_get(L + L_CTL);
    {
      const int mt64 = 2 * mt + wm;
      f32x2 nm[2], K1[2], K2[2], iR2[2];
#pragma unroll
      for (int t = 0; t < 2; ++t) {
        const f32x2 st = *(LAS f32x2*)(L + L_ST + (tok + 32 * t) * 8);
        const float r = st[1], k1 = -2.3022082f * r, k2 = (-2.3022082f * 0.044715f) * r * r * r, ir = __builtin_amdgcn_rcpf(r * r);
        nm[t] = f32x2{-st[0], -st[0]}; K1[t] = f32x2{k1, k1}; K2[t] = f32x2{k2, k2}; iR2[t] = f32x2{ir, ir};
      }
#pragma unroll
      for (int i = 0; i < 3; ++i) {
        LAS char* cp = L + L_CS + (16 * (3 * wn + i) + 4 * h) * 4;
        const f32x4 cg0 = *(LAS f32x4*)(cp), cg1 = *(LAS f32x4*)(cp + 32), cu0 = *(LAS f32x4*)(cp + 384), cu1 = *(LAS f32x4*)(cp + 416);
        const unsigned vo = 3 * wn + i < nblk ? lane * 16 : OOB;
#pragma unroll
        for (int t = 0; t < 2; ++t) {
          v4u o;
#pragma unroll
          for (int e = 0; e < 4; ++e) {
            const f32x4 cgv = e < 2 ? cg0 : cg1, cuv = e < 2 ? cu0 : cu1;
            const f32x2 cg = {cgv[2 * (e & 1)], cgv[2 * (e & 1) + 1]}, cu = {cuv[2 * (e & 1)], cuv[2 * (e & 1) + 1]};
            const f32x2 ag = {acc[t][i][2 * e], acc[t][i][2 * e + 1]}, au = {acc[t][i][8 + 2 * e], acc[t][i][9 + 2 * e]};
            const f32x2 G = __builtin_elementwise_fma(nm[t], cg, ag), U = __builtin_elementwise_fma(nm[t], cu, au);
            const f32x2 ww = G * __builtin_elementwise_fma(G * G, K2[t], K1[t]);
            const f32x2 ex = {__builtin_amdgcn_exp2f(ww[0]), __builtin_amdgcn_exp2f(ww[1])};
            const f32x2 dn = __builtin_elementwise_fma(ex, iR2[t], iR2[t]);
            const f32x2 rc = {__builtin_amdgcn_rcpf(dn[0]), __builtin_amdgcn_rcpf(dn[1])};
            const f32x2 hv = (G * U) * rc;
            o[e] = pk2(hv[0], hv[1]);
          }
          st128(o, rws, vo, WS_HT + ((mt64 * NFB + ft * 6 + 3 * wn + i) * 2 + t) * 1024);
          {
            const int q0 = (2 * i + t) * NEPI;
#pragma unroll
            for (int qq = q0; qq < q0 + NEPI; ++qq) wdf[qq >> 1][qq & 1] = ld128(rws, lane * 16, WS_WDT + ((nt * NFB + k0) * 2 + qq) * 1024, 1);
#pragma unroll
            for (int g4 = 0; g4 < NEPI; ++g4) { SGB(0x2, 52 / (NEPI + 1)); SGB(0x20, 1); }
            SCHED();
          }
        }
      }
      lds_set(L + L_FREE + 4 * w, 9u);
      STAMP(4);
      VMW(0);
      flag_set(rws, WS_HF + (mt64 * 32 + ft * 2 + wn) * FL, T, lane, 3 * wn < nblk);
    }
    __builtin_amdgcn_s_setprio(0);
    STAMP(5);
#pragma unroll
    for (int qq = 6 * NEPI; qq < 22; ++qq) wdf[qq >> 1][qq & 1] = ld128(rws, lane * 16, WS_WDT + ((nt * NFB + k0) * 2 + qq) * 1024, 1);
    SCHED();
    wait_free(L, 8);
  } else {
    const int chl = (w - 4) >> 1, d0 = 64 * xcd + 2 * j, d = d0 + chl;
    const int m0 = 1 + (w & 1) * 128 + lane;
    auto load_wd = [&]() AI {
#pragma unroll
      for (int i = 0; i < 11; ++i)
#pragma unroll
        for (int e = 0; e < 2; ++e) wdf[i][e] = ld128(rws, i < nk ? lane * 16 : OOB, WS_WDT + ((nt * NFB + k0 + i) * 2 + e) * 1024, 1);
      SCHED();
    };
    if (w < 6) {
      __builtin_amdgcn_s_setprio(3);
      if (w == 5) {
        wait_word(L + L_ORD, 1);
        stage(1);
        stage(2);
        VMW(40);
        lds_set(L + L_FULL + 4, 1u);
        STAMP(12);
      }
      VMW(0);
      lds_set(L + L_FULL + 8 * (w - 4), 1u);
      STAMP(3 + 2 * w);
#pragma unroll 1
      for (int n = w - 1; n < 8; n += 2) {
        wait_free(L, n - 2);
        stage(n);
        lds_set(L + L_ISS + 4 * (n & 1), n);
        if (n >= 6) break;
        VMW(0);
        lds_set(L + L_FULL + 4 * n, 1u);
        STAMP(11 + n);
      }
      VMW(0);
      lds_set(L + L_FULL + 4 * (11 - w), 1u);
      STAMP(17);
      __builtin_amdgcn_s_setprio(0);
    } else {
      const int hw = w - 6;
      unsigned E1 = 0;
      if (hw) E1 = flag_get(ws, WS_EPOCH) + 1u;
      v4u pl[16];
      if (HWAIT) wait_word(L + L_ORD, 1);
      if (hw) {
        const rsrc_t rx = mkrs(x, S * D * 2);
#pragma unroll
        for (int i = 0; i < 8; ++i) pl[i] = ld128(rx, (lane >> 3) * 1024 + (lane & 7) * 16, (mt * 128 + ft * 8) * 1024 + i * 128, 0);
        const rsrc_t rw = mkrs(wd, F * D * 2);
#pragma unroll
        for (int u = 0; u < 3; ++u) {
          const int uid = b + 256 * u;
#pragma unroll
          for (int i = 0; i < 2; ++i)
            dma(rw, L + L_WS + (2 * u + i) * 1024, uid < 688 ? (lane >> 3) * 1024 + (lane & 7) * 16 : OOB, (16 * (uid >> 3) + 8 * i) * 1024 + (uid & 7) * 128);
        }
        SCHED();
        lds_set(L + L_CTL, E1);
        lds_set(L + L_KCT, 1u);
      } else {
        const int c = lane >> 1, cc = c < 12 ? c : c - 12;
        const bool ok = lane < 48 && cc < 2 * nblk;
        const char* base = (const char*)(c < 12 ? wg : wu) + (ok ? (size_t)((32 * mt + 16 * (lane & 1)) * WROW + ft * 192 + cc * 16) : 0);
#pragma unroll
        for (int i = 0; i < 16; ++i) pl[i] = *(const v4u*)(base + i * WROW);
        SCHED();
      }
      __builtin_amdgcn_s_setprio(HPRIO);
      wait_word(L + L_KCT, 1);
      E1 = lds_get(L + L_CTL);
      if (hw) {
        float s0 = 0.f, s1 = 0.f;
#pragma unroll
        for (int i = 0; i < 8; ++i)
#pragma unroll
          for (int e = 0; e < 4; ++e) { s0 = sadd(s0, bflo(pl[i][e])); s1 = sadd(s1, bfhi(pl[i][e])); }
        const float mean = sum8(s0 + s1) * (1.0f / D);
        float q0 = 0.f, q1 = 0.f;
#pragma unroll
        for (int i = 0; i < 8; ++i)
#pragma unroll
          for (int e = 0; e < 4; ++e) {
            const float a0 = ssub(bflo(pl[i][e]), mean), a1 = ssub(bfhi(pl[i][e]), mean);
            q0 = sfma(a0, a0, q0); q1 = sfma(a1, a1, q1);
          }
        const float sd = rbf(__builtin_sqrtf(rbf(rbf(sum8(q0 + q1) * (1.0f / D)) + 1e-5f)));
        st64(v2u{f2u(rbf(mean)), f2u(1.0f / sd)}, rws, (lane & 7) == 0 ? WS_STATS + (mt * 128 + ft * 8 + (lane >> 3)) * 8 : OOB, 0);
        LAS char* tp = L + L_WS + (4 * h + ((lane & 15) >> 2)) * 128 + (4 * ((lane >> 4) & 1) + (lane & 3)) * 16;
        VMW(1);
#pragma unroll
        for (int q = 0; q < 6; ++q) {
          const int uid = b + 256 * (q >> 1);
          const v2u lo = __builtin_bit_cast(v2u, __builtin_amdgcn_ds_read_tr16_b64_v4i16((LAS s16x4*)(tp + (q >> 1) * 2048 + (q & 1) * 8)));
          const v2u hi = __builtin_bit_cast(v2u, __builtin_amdgcn_ds_read_tr16_b64_v4i16((LAS s16x4*)(tp + (q >> 1) * 2048 + (q & 1) * 8 + 1024)));
          st128(v4u{lo.x, lo.y, hi.x, hi.y}, rws, uid < 688 ? lane * 16 : OOB, WS_WDT + (((uid & 7) * NFB + (uid >> 3)) * 2 + (q & 1)) * 1024);
        }
      } else {
        float cs[8];
#pragma unroll
        for (int e = 0; e < 8; ++e) cs[e] = 0.f;
#pragma unroll
        for (int i = 0; i < 16; ++i)
#pragma unroll
          for (int e = 0; e < 4; ++e) { cs[2 * e] = sadd(cs[2 * e], bflo(pl[i][e])); cs[2 * e + 1] = sadd(cs[2 * e + 1], bfhi(pl[i][e])); }
        float o[4];
#pragma unroll
        for (int e = 0; e < 4; ++e) {
          const float mine = (lane & 1) ? cs[4 + e] : cs[e], give = (lane & 1) ? cs[e] : cs[4 + e];
          o[e] = mine + dpp<0xB1>(give);
        }
        st128(v4u{f2u(o[0]), f2u(o[1]), f2u(o[2]), f2u(o[3])}, rws, (lane < 48 && nblk) ? lane * 16 : OOB, WS_CSP + (ft * 16 + mt) * 768);
      }
      if (!hw) {
        wait_word(L + L_KVD, 4);
#pragma unroll
        for (int i = 0; i < 8; ++i)
          st128(*(LAS v4u*)(L + L_KO + i * 1024 + lane * 16), rws, WS_KVR + (64 * xcd + (lane >> 3)) * 4096 + (1984 - 64 * j) * 2 + (lane & 7) * 16, i * 8 * 4096);
      }
      STAMP(12);
      __builtin_amdgcn_s_setprio(0);
      v4u hl[2][2][2];
#pragma unroll
      for (int c = 0; c < 2; ++c) {
        const rsrc_t rh = mkrs(hk + (d0 + c) * S, S * 2);
#pragma unroll
        for (int i = 0; i < 2; ++i) { hl[c][i][0] = ld128(rh, (m0 + 64 * i - 2) * 16, 0, 0); hl[c][i][1] = ld128(rh, (m0 + 64 * i - 1) * 16, 0, 0); }
      }
      SCHED();
      arrive(L, L_PCT, 2, rws, WS_PF + b * FL, E1, lane);
      STAMP(13);
      wait_word(L + L_PCT, 2);
      build_hc(L + L_HC0, m0, hl[0][0][0], hl[0][0][1]);
      build_hc(L + L_HC0, m0 + 64, hl[0][1][0], hl[0][1][1]);
      STAMP(14);
      {
        const unsigned po = WS_PF + (hw ? (lane < 16 ? wg_of_tile(mt, lane) : wg_of_tile(lane & 15, ft)) : (lane & 31) * 8 + (lane < 32 ? xcd : nt)) * FL;
        bool ok = false;
#pragma unroll 1
        for (int n = POLLST; n < 8 && !ok; ++n) {
          wait_word(L + L_ISS + 4 * (n & 1), n);
          ok = all_of(flag_get(ws, po) == E1);
        }
        if (!ok) poll_words<PSLEEP>(ws, po, E1);
        ORDER_BEHIND_POLL();
      }
      STAMP(15);
      if (hw) {
#pragma unroll
        for (int i = 0; i < 16; ++i) pl[i] = ld128(rws, (lane < 48 && nblk) ? lane * 16 : OOB, WS_CSP + (ft * 16 + i) * 768, 1);
        const v4u sv = ld128(rws, lane * 16, WS_STATS + mt * 1024, 1);
        SCHED();
        lds_add(L + L_POK, lane);
        f32x4 cs = {0.f, 0.f, 0.f, 0.f};
#pragma unroll
        for (int i = 0; i < 16; ++i)
#pragma unroll
          for (int e = 0; e < 4; ++e) cs[e] = sadd(cs[e], u2f(pl[i][e]));
        if (lane < 48) *(LAS f32x4*)(L + L_CS + lane * 16) = cs;
        *(LAS v4u*)(L + L_ST + lane * 16) = sv;
        lds_set(L + L_EPI, 1u);
      } else {
#pragma unroll
        for (int i = 0; i < 8; ++i) pl[i] = ld128(rws, lane * 16, WS_KVR + d0 * 4096 + i * 1024, 1);
        SCHED();
        lds_add(L + L_POK, lane);
#pragma unroll
        for (int i = 0; i < 4; ++i) *(LAS v4u*)(L + L_UR0 + i * 1024 + lane * 16) = pl[i];
      }
      STAMP(16);
      wait_free(L, 6);
      build_hc(L + L_HC1, m0, hl[1][0][0], hl[1][0][1]);
      build_hc(L + L_HC1, m0 + 64, hl[1][1][0], hl[1][1][1]);
      {
        LAS char* hz = L + (hw ? L_HC1 : L_HC0);
        LAS char* uz = L + (hw ? L_UR1 : L_UR0);
        if (lane < 8) *(LAS v4u*)(hz + lane * HC_P) = v4u{0, 0, 0, 0};
        else if (lane < 40) *(LAS v4u*)(uz + 4096 + (lane - 8) * 16) = v4u{0, 0, 0, 0};
        if (!hw) {
#pragma unroll
          for (int i = 0; i < 4; ++i) *(LAS v4u*)(L + L_UR1 + i * 1024 + lane * 16) = pl[4 + i];
        }
      }
      lds_add(L + L_CVK, lane);
    }
    LAS char* hc = L + (chl ? L_HC1 : L_HC0);
    LAS char* ur = L + (chl ? L_UR1 : L_UR0);
    const float Dd = bflo(sD[d]);
    STAMP(18);
    wait_word(L + L_POK, 2);
    wait_word(L + L_ISS + 4, 7);
    load_wd();
    STAMP(2);
    wait_word(L + L_CVK, 2);
    wait_free(L, CONVGO);
    STAMP(3);
    T = lds_get(L + L_CTL);
    __builtin_amdgcn_s_setprio(2);
    if (w & 1) conv_part<6, 8>(hc, ur, lane, Dd, rws, d);
    else conv_part<0, 6>(hc, ur, lane, Dd, rws, d);
    __builtin_amdgcn_s_setprio(0);
    STAMP(4);
    arrive(L, L_ZCT, 4, rws, WS_ZF + (d0 >> 1) * FL, T, lane);
    STAMP(5);
  }

  {
    const int tb = w >> 2, q = w & 3;
    const int tl = 32 * tb + (lane & 31), tok = 64 * m2 + tl, dd = 64 * nt + 16 * q + 8 * h;
    const v4u xv = *(const v4u*)(x + tok * D + dd);
    poll_words(ws, WS_HF + (m2 * 32 + umin(k0 / 3 + (lane & 7), (k0 + nk - 1) / 3)) * FL, T);
    STAMP(6);
    const v2u stv2 = __builtin_amdgcn_raw_buffer_load_b64(rws, WS_STATS + tok * 8, 0, SC1);
    v4u hf[11][2];
#pragma unroll
    for (int i = 0; i < 8; ++i)
#pragma unroll
      for (int t = 0; t < 2; ++t) hf[i][t] = ld128(rws, lane * 16, WS_HT + ((m2 * NFB + k0 + i) * 2 + t) * 1024, 1);
    SCHED();
    const unsigned zfo = WS_ZF + (32 * nt + 8 * q + (lane & 7)) * FL;
    const unsigned zf = flag_get(ws, zfo);
    SCHED();
#pragma unroll
    for (int i = 8; i < 11; ++i)
#pragma unroll
      for (int t = 0; t < 2; ++t) hf[i][t] = ld128(rws, i < nk ? lane * 16 : OOB, WS_HT + ((m2 * NFB + k0 + i) * 2 + t) * 1024, 1);
    SCHED();
    f32x16 acc[2][2];
#pragma unroll
    for (int t = 0; t < 2; ++t)
#pragma unroll
      for (int e = 0; e < 2; ++e)
#pragma unroll
        for (int r = 0; r < 16; ++r) acc[t][e][r] = 0.f;
#pragma unroll
    for (int i = 0; i < 8; ++i)
#pragma unroll
      for (int t = 0; t < 2; ++t)
#pragma unroll
        for (int e = 0; e < 2; ++e) acc[t][e] = mfma32(asb(wdf[i][e]), asb(hf[i][t]), acc[t][e]);
    SCHED();
    if (!all_of(zf == T)) poll_words(ws, zfo, T);
    ORDER_BEHIND_POLL();
    const v4u zv = ld128(rws, WS_ZT + (64 * nt + 16 * q + (lane >> 2)) * 4096 + (64 * m2 + 32 * tb + 8 * (lane & 3)) * 2, 0, 1);
    SCHED();
#pragma unroll
    for (int i = 8; i < 11; ++i)
#pragma unroll
      for (int t = 0; t < 2; ++t)
#pragma unroll
        for (int e = 0; e < 2; ++e) acc[t][e] = mfma32(asb(wdf[i][e]), asb(hf[i][t]), acc[t][e]);
    STAMP(7);
#pragma unroll
    for (int t = 0; t < 2; ++t)
#pragma unroll
      for (int qq = 0; qq < 4; ++qq) {
        const v4u o = {pk2(acc[t][0][4 * qq], acc[t][0][4 * qq + 1]), pk2(acc[t][0][4 * qq + 2], acc[t][0][4 * qq + 3]),
                       pk2(acc[t][1][4 * qq], acc[t][1][4 * qq + 1]), pk2(acc[t][1][4 * qq + 2], acc[t][1][4 * qq + 3])};
        *(LAS v4u*)(L + L_RED + w * 8192 + (32 * t + (lane & 31)) * 128 + (((2 * qq + h) ^ (lane & 7)) << 4)) = o;
      }
    {
      *(LAS v4u*)(L + L_ZS + w * 1024 + lane * 16) = zv;
      LAS char* zp = L + L_ZS + w * 1024 + (8 * h + ((lane & 15) >> 2)) * 64 + (16 * ((lane >> 4) & 1) + 4 * (lane & 3)) * 2;
      const v2u z0 = __builtin_bit_cast(v2u, __builtin_amdgcn_ds_read_tr16_b64_v4i16((LAS s16x4*)(zp)));
      const v2u z1 = __builtin_bit_cast(v2u, __builtin_amdgcn_ds_read_tr16_b64_v4i16((LAS s16x4*)(zp + 256)));
      const unsigned zz[4] = {z0.x, z0.y, z1.x, z1.y};
      const float m = u2f(stv2.x), r = u2f(stv2.y);
      v4u o;
#pragma unroll
      for (int e = 0; e < 4; ++e) {
        const float x0 = bflo(xv[e]), x1 = bfhi(xv[e]);
        const unsigned c = pk2(ssub(x0, m), ssub(x1, m));
        const unsigned xn = pk2(smul(bflo(c), r), smul(bfhi(c), r));
        const unsigned a = pk2(smul(bflo(xn), bflo(zz[e])), smul(bfhi(xn), bfhi(zz[e])));
        o[e] = pk2(sadd(x0, bflo(a)), sadd(x1, bfhi(a)));
      }
      *(LAS v4u*)(L + L_TP + tl * 128 + (((2 * q + h) ^ (lane & 7)) << 4)) = o;
    }
    BAR();
    STAMP(8);
    {
      LAS char* rp = L + L_RED + w * 1024 + (lane >> 3) * 128 + (((lane & 7) ^ (lane >> 3)) << 4);
      f32x2 f[4];
#pragma unroll
      for (int e = 0; e < 4; ++e) f[e] = f32x2{0.f, 0.f};
#pragma unroll
      for (int ww = 0; ww < 8; ++ww) {
        const v4u p = *(LAS v4u*)(rp + ww * 8192);
#pragma unroll
        for (int e = 0; e < 4; ++e) f[e] += f32x2{bflo(p[e]), bfhi(p[e])};
      }
      const v4u tp = *(LAS v4u*)(rp + L_TP);
      v4u o;
#pragma unroll
      for (int e = 0; e < 4; ++e) {
        const unsigned ff = pk2(f[e][0], f[e][1]);
        o[e] = pk2(bflo(tp[e]) + bflo(ff), bfhi(tp[e]) + bfhi(ff));
      }
      *(v4u*)(out + (64 * m2 + 8 * w + (lane >> 3)) * D + 64 * nt + 8 * (lane & 7)) = o;
    }
    STAMP(9);
    if (b == 0 && w == 7) {
#pragma unroll 1
      for (int i = 0; i < 2; ++i) poll_words2(ws, WS_PF + (128 * i + lane) * FL, WS_PF + (128 * i + 64 + lane) * FL, T);
      if (lane == 0) word_set(ws, WS_EPOCH, T);
    }
  }
}

extern "C" void kernel_launch(void* const* d_in, const int* in_sizes, int n_in, void* d_out, int out_size, void* d_ws, size_t ws_size,
                              hipStream_t stream) {
  fused_kernel<<<256, 512, 0, stream>>>((const u16*)d_in[0], (const u16*)d_in[7], (const u16*)d_in[8], (char*)d_ws, (const u16*)d_in[1],
                                        (const u16*)d_in[2], (const u16*)d_in[3], (const u16*)d_in[4], (const u16*)d_in[5], (const u16*)d_in[9],
                                        (u16*)d_out);
}
```

```cpp
#define CODEPF 7
#include <hip/hip_runtime.h>
#include <hip/hip_bf16.h>
#include <stdint.h>

typedef unsigned short u16;
typedef unsigned v4u __attribute__((ext_vector_type(4)));
typedef unsigned v2u __attribute__((ext_vector_type(2)));
typedef short bf16x8 __attribute__((ext_vector_type(8)));
typedef short s16x4 __attribute__((ext_vector_type(4)));
typedef float f32x16 __attribute__((ext_vector_type(16)));
typedef float f32x4 __attribute__((ext_vector_type(4)));
typedef float f32x2 __attribute__((ext_vector_type(2)));
typedef __bf16 bf2 __attribute__((ext_vector_type(2)));
#define LAS __attribute__((address_space(3)))
#define GAS __attribute__((address_space(1)))
#define DI __device__ __forceinline__
#define AI __attribute__((always_inline))
typedef __amdgpu_buffer_rsrc_t rsrc_t;

constexpr int S = 2048, D = 512, F = 1376, NFB = 86;
constexpr int WROW = F * 2;
constexpr unsigned FL = 128;
constexpr unsigned WS_EPOCH = 0, WS_PF = 1024, WS_KF = WS_PF + 256 * FL, WS_ZF = WS_KF + 256 * FL, WS_HF = WS_ZF + 256 * FL;
constexpr unsigned WS_STATS = WS_HF + 1024 * FL;
constexpr unsigned WS_CSP = WS_STATS + 16384;
constexpr unsigned WS_KVR = 524288;
static_assert(WS_CSP + 15 * 16 * 768 <= WS_KVR, "ws");
constexpr unsigned WS_ZT = WS_KVR + 2097152;
constexpr unsigned WS_WDT = WS_ZT + 2097152;
constexpr unsigned WS_HT = WS_WDT + 8 * 86 * 2048;
constexpr unsigned WS_END = WS_HT + 32 * 86 * 2048;
constexpr unsigned WS_STAMP = WS_END;
constexpr int SLOT = 40960, L_XW = 16384, L_UP = 28672;
constexpr int L_CS = 122880, L_ST = L_CS + 768, L_CTL = L_CS + 1792;
constexpr int L_ZCT = L_CTL + 4, L_CVK = L_CTL + 8, L_EPI = L_CTL + 12, L_FREE = L_CTL + 16, L_FULL = L_CTL + 32, L_POK = L_CTL + 64, L_ISS = L_CTL + 88,
              L_STG = L_CTL + 72, L_PCT = L_CTL + 76, L_KCT = L_CTL + 80, L_ORD = L_CTL + 84, L_KVD = L_CTL + 96;
constexpr int HC_P = 4160, HC_B = 8 * HC_P;
constexpr int L_HC0 = 124928, L_UR0 = L_HC0 + HC_B, L_HC1 = 2 * SLOT, L_UR1 = L_HC1 + HC_B;
constexpr int L_KS = L_HC0, L_VS = L_KS + 9216, L_WS = L_VS + 8192, L_KO = L_WS + 6144;
constexpr int L_RED = 0, L_TP = 65536, L_ZS = 73728;
constexpr int LDS_BYTES = 163840;
static_assert(L_UR0 + 4608 <= LDS_BYTES && L_UR1 + 4608 <= 3 * SLOT, "lds");
#define NEPI 2
#define L1SLEEP 10
#define EARLYWD 1
#define PSLEEP 4
#define HSLEEP 20
#define PFSLEEP 20
#define POLLST 5
#define CONVGO 9
#define DATAPF 0
#define SCALAREPI 0
#define CSST 6
#define KVST 1
#define HWAIT 1
#define OUTAUX 0
#define HPRIO 3
constexpr int SC1 = 16;
constexpr unsigned OOB = 0x80000000u;
constexpr unsigned SPIN_CAP = 1u << 14;

#define BAR() asm volatile("s_waitcnt lgkmcnt(0)\n\ts_barrier" ::: "memory")
#define BARS(i) do { STAMP(10 + (i)); BAR(); } while (0)
#define SGB(m, n) __builtin_amdgcn_sched_group_barrier(m, n, 0)
#define SCHED() __builtin_amdgcn_sched_barrier(0)
#define VMW(n) asm volatile("s_waitcnt vmcnt(%0)" ::"i"(n) : "memory")
#define RLX_AGENT __ATOMIC_RELAXED, __HIP_MEMORY_SCOPE_AGENT

#define STAMPCLK() __builtin_amdgcn_s_memrealtime()
#define STAMP(i)

DI float u2f(unsigned u) { return __builtin_bit_cast(float, u); }
DI unsigned f2u(float f) { return __builtin_bit_cast(unsigned, f); }
DI bool all_of(bool c) { return __builtin_amdgcn_ballot_w64(!c) == 0; }
template <int CTRL> DI float dpp(float v) { return u2f(__builtin_amdgcn_update_dpp(0u, f2u(v), CTRL, 0xf, 0xf, true)); }
DI float sum8(float v) { v += dpp<0xB1>(v); v += dpp<0x4E>(v); v += dpp<0x141>(v); return v; }
DI unsigned umin(unsigned a, unsigned b) { return a < b ? a : b; }
DI float sadd(float a, float b) { float d; asm("v_add_f32 %0, %1, %2" : "=v"(d) : "v"(a), "v"(b)); return d; }
DI float ssub(float a, float b) { float d; asm("v_sub_f32 %0, %1, %2" : "=v"(d) : "v"(a), "v"(b)); return d; }
DI float smul(float a, float b) { float d; asm("v_mul_f32 %0, %1, %2" : "=v"(d) : "v"(a), "v"(b)); return d; }
DI float sfma(float a, float b, float c) { float d; asm("v_fma_f32 %0, %1, %2, %3" : "=v"(d) : "v"(a), "v"(b), "v"(c)); return d; }
DI float bflo(unsigned u) { return u2f(u << 16); }
DI float bfhi(unsigned u) { return u2f(u & 0xffff0000u); }
DI unsigned pk2(float a, float b) { f32x2 v = {a, b}; return __builtin_bit_cast(unsigned, __builtin_convertvector(v, bf2)); }
DI float rbf(float a) { return bflo(pk2(a, a)); }
DI rsrc_t mkrs(const void* p, unsigned n) { return __builtin_amdgcn_make_buffer_rsrc((void*)p, 0, n, 0x00020000); }
DI v4u ld128(rsrc_t r, unsigned vo, unsigned so, int aux) { return aux ? __builtin_amdgcn_raw_buffer_load_b128(r, vo, so, SC1) : __builtin_amdgcn_raw_buffer_load_b128(r, vo, so, 0); }
DI unsigned ld32(rsrc_t r, unsigned vo, unsigned so) { return __builtin_amdgcn_raw_buffer_load_b32(r, vo, so, 0); }
DI void st128(v4u v, rsrc_t r, unsigned vo, unsigned so) { __builtin_amdgcn_raw_buffer_store_b128(v, r, vo + so, 0, SC1); }
DI void st64(v2u v, rsrc_t r, unsigned vo, unsigned so) { __builtin_amdgcn_raw_buffer_store_b64(v, r, vo, so, SC1); }
DI void dma(rsrc_t r, LAS char* dst, unsigned vo, unsigned so) { __builtin_amdgcn_raw_ptr_buffer_load_lds(r, (LAS void*)dst, 16, vo, so, 0, 0); }
DI bf16x8 asb(v4u v) { return __builtin_bit_cast(bf16x8, v); }
DI f32x16 mfma32(bf16x8 a, bf16x8 b, f32x16 c) { return __builtin_amdgcn_mfma_f32_32x32x16_bf16(a, b, c, 0, 0, 0); }
DI f32x4 mfma16(bf16x8 a, bf16x8 b, f32x4 c) { return __builtin_amdgcn_mfma_f32_16x16x32_bf16(a, b, c, 0, 0, 0); }
DI void word_set(char* ws, unsigned off, unsigned T) { __hip_atomic_store((GAS unsigned*)(ws + off), T, RLX_AGENT); }
DI void flag_set(rsrc_t rws, unsigned off, unsigned T, int lane, bool on = true) { st128(v4u{T, T, T, T}, rws, (lane < 8 && on) ? lane * 16 : OOB, off); }
DI unsigned flag_get(char* ws, unsigned off) { return __hip_atomic_load((GAS unsigned*)(ws + off), RLX_AGENT); }
#define ORDER_BEHIND_POLL() __builtin_amdgcn_fence(__ATOMIC_ACQUIRE, "wavefront")

template <int SLEEP = 3>
DI void poll_words(char* ws, unsigned off, unsigned T) {
#pragma nounroll
  for (unsigned spins = 0; spins < SPIN_CAP; ++spins) {
    const unsigned v = flag_get(ws, off);
    if (all_of(v == T)) break;
    __builtin_amdgcn_s_sleep(SLEEP);
  }
  ORDER_BEHIND_POLL();
}
template <int SLEEP>
DI void poll_deep(char* ws, unsigned off, unsigned T, unsigned v0) {
#pragma nounroll
  for (unsigned spins = 0; spins < SPIN_CAP; ++spins) {
    const unsigned v1 = flag_get(ws, off);
    if (all_of(v0 == T)) break;
    __builtin_amdgcn_s_sleep(SLEEP);
    v0 = flag_get(ws, off);
    if (all_of(v1 == T)) break;
    __builtin_amdgcn_s_sleep(SLEEP);
  }
  ORDER_BEHIND_POLL();
}
DI void poll_words2(char* ws, unsigned off0, unsigned off1, unsigned T) {
#pragma nounroll
  for (unsigned spins = 0; spins < SPIN_CAP; ++spins) {
    const unsigned v0 = flag_get(ws, off0), v1 = flag_get(ws, off1);
    if (all_of(v0 == T && v1 == T)) break;
    __builtin_amdgcn_s_sleep(6);
  }
  ORDER_BEHIND_POLL();
}
DI int wg_of_tile(int mt, int ft) {
  const int mh = mt >> 3, ml = mt & 7, idx = ft * 8 + ml;
  const int fq = ft < 14 ? idx / 28 : ml >> 1;
  const int j = ft < 14 ? idx - 28 * fq : 28 + 2 * (ft - 14) + (ml & 1);
  return j * 8 + fq * 2 + mh;
}
DI unsigned lds_get(LAS char* p) { return __builtin_amdgcn_readfirstlane(*(volatile LAS unsigned*)p); }
DI void lds_set(LAS char* p, unsigned v) { *(volatile LAS unsigned*)p = v; }
DI void wait_word(LAS char* p, unsigned want) {
#pragma nounroll
  for (unsigned spins = 0; spins < (1u << 18) && lds_get(p) < want; ++spins) __builtin_amdgcn_s_sleep(1);
  asm volatile("" ::: "memory");
}
DI void wait_free(LAS char* L, unsigned want) {
#pragma nounroll
  for (unsigned spins = 0; spins < (1u << 18); ++spins) {
    const v4u f = *(volatile LAS v4u*)(L + L_FREE);
    const unsigned m = __builtin_amdgcn_readfirstlane(umin(umin(f.x, f.y), umin(f.z, f.w)));
    if (m >= want) break;
    __builtin_amdgcn_s_sleep(1);
  }
  asm volatile("" ::: "memory");
}
DI void lds_add(LAS char* p, int lane) {
  if (lane == 0) __hip_atomic_fetch_add((LAS unsigned*)p, 1u, __ATOMIC_RELAXED, __HIP_MEMORY_SCOPE_WORKGROUP);
}
DI void arrive(LAS char* L, int ctr, unsigned n, rsrc_t rws, unsigned flag, unsigned T, int lane) {
  VMW(0);
  unsigned old = 0;
  if (lane == 0) old = __hip_atomic_fetch_add((LAS unsigned*)(L + ctr), 1u, __ATOMIC_RELAXED, __HIP_MEMORY_SCOPE_WORKGROUP);
  if (__builtin_amdgcn_readfirstlane(old) == n - 1) flag_set(rws, flag, T, lane);
}

DI void build_hc(LAS char* hc, int m, v4u lo, v4u hi) {
  unsigned d[8] = {lo.x, lo.y, lo.z, lo.w, hi.x, hi.y, hi.z, hi.w};
  unsigned o[7];
#pragma unroll
  for (int i = 0; i < 7; ++i) o[i] = __builtin_amdgcn_alignbit(d[i + 1], d[i], 16);
#pragma unroll
  for (int c = 0; c < 8; ++c) {
    const int sg = c + 1;
    v4u v;
    if (sg & 1) { const int i = (sg - 1) / 2; v = v4u{o[i], o[i + 1], o[i + 2], o[i + 3]}; }
    else { const int i = sg / 2; v = v4u{d[i], d[i + 1], d[i + 2], d[i + 3]}; }
    *(LAS v4u*)(hc + c * HC_P + m * 16) = v;
  }
}

template <int N0, int N1>
DI void conv_part(LAS char* hc, LAS char* ur, int lane, float Dd, rsrc_t rws, int d) {
  constexpr int NQ = N1;
  const int b = lane & 15, g = lane >> 4;
  LAS char* ab = hc + (b & 7) * HC_P + (g + (b >> 3)) * 16;
  LAS char* bb = ur + 2 * (2032 - 16 * b + 8 * g) - 512 * (NQ - 1);
  f32x4 acc[N1 - N0];
#pragma unroll
  for (int n = 0; n < N1 - N0; ++n) acc[n] = f32x4{0.f, 0.f, 0.f, 0.f};
  bf16x8 A[2][NQ], B[2][NQ];
#pragma unroll
  for (int q = 0; q < NQ; ++q) {
    A[0][q] = *(LAS bf16x8*)(ab + 512 * q);
    B[0][N0 ? NQ - 1 - q : q] = *(LAS bf16x8*)(bb + 512 * (N0 ? q : NQ - 1 - q));
  }
  SCHED();
#pragma unroll
  for (int r = 0; r < 8; ++r) {
    const int c = r & 1;
    if (r < 7) {
#pragma unroll
      for (int q = 0; q < NQ; ++q) {
        A[c ^ 1][q] = *(LAS bf16x8*)(ab + 512 * q + 64 * (r + 1));
        B[c ^ 1][N0 ? NQ - 1 - q : q] = *(LAS bf16x8*)(bb + 512 * (N0 ? q : NQ - 1 - q) + 64 * (r + 1));
      }
    }
    if (N0) {
#pragma unroll
      for (int q = 0; q < NQ; ++q) {
        acc[1] = mfma16(A[c][q], B[c][7 - q], acc[1]);
        if (q) acc[0] = mfma16(A[c][q - 1], B[c][7 - q], acc[0]);
      }
#pragma unroll
      for (int g2 = 0; g2 < 15; ++g2) { SGB(0x100, 1); SGB(8, 1); }
      SGB(0x100, 1);
    } else {
#pragma unroll
      for (int i = 0; i < NQ; ++i) {
#pragma unroll
        for (int p = 0; p < i; ++p)
          if (i + p < NQ) {
            acc[(i + p) % (N1 - N0)] = mfma16(A[c][i], B[c][p], acc[(i + p) % (N1 - N0)]);
            acc[(i + p) % (N1 - N0)] = mfma16(A[c][p], B[c][i], acc[(i + p) % (N1 - N0)]);
          }
        if (2 * i < NQ) acc[(2 * i) % (N1 - N0)] = mfma16(A[c][i], B[c][i], acc[(2 * i) % (N1 - N0)]);
      }
#pragma unroll
      for (int g2 = 0; g2 < 12; ++g2) { SGB(8, 1); SGB(0x100, 1); }
      SGB(8, 9);
    }
    SCHED();
  }
#pragma unroll
  for (int n = N0; n < N1; ++n) {
    const int s = 256 * n + 16 * b + 4 * g;
    v2u uu = *(LAS v2u*)(ur + 2 * (2044 - s));
    const f32x4 y = acc[n - N0];
    v2u o;
    o.x = pk2(sfma(bfhi(uu.y), Dd, y[0]), sfma(bflo(uu.y), Dd, y[1]));
    o.y = pk2(sfma(bfhi(uu.x), Dd, y[2]), sfma(bflo(uu.x), Dd, y[3]));
    st64(o, rws, WS_ZT + d * 4096 + s * 2, 0);
  }
}

__global__ __launch_bounds__(512, 2) void fused_kernel(const u16* __restrict__ x, const u16* __restrict__ wg, const u16* __restrict__ wu,
                                                            char* ws, const u16* __restrict__ kin, const u16* __restrict__ vin,
                                                            const u16* __restrict__ sk, const u16* __restrict__ hk, const u16* __restrict__ sD,
                                                            const u16* __restrict__ wd, u16* __restrict__ out) {
  __shared__ __attribute__((aligned(16))) char smem[LDS_BYTES];
  LAS char* L = (LAS char*)smem;
  const int tid = threadIdx.x, lane = tid & 63, w = __builtin_amdgcn_readfirstlane(tid >> 6), b = blockIdx.x;
  const int xcd = b & 7, j = b >> 3;
  STAMP(0);
  const int idx = (xcd >> 1) * 28 + j;
  const int ft = j < 28 ? idx >> 3 : 14 + ((j - 28) >> 1);
  const int mt = (xcd & 1) * 8 + (j < 28 ? idx & 7 : (xcd & 6) + (j & 1));
  const int nblk = ft < 14 ? 6 : (ft == 14 ? 2 : 0);
  unsigned vx[2], vw[3];
#pragma unroll
  for (int p = 0; p < 2; ++p) vx[p] = (lane >> 3) * 1024 + (((lane & 7) ^ (4 * p + (lane >> 4))) << 4);
#pragma unroll
  for (int n = 0; n < 3; ++n) {
    const int G = 4 * n + (lane >> 4), kq = (G * 86) >> 8, uc = G - 3 * kq;
    const unsigned v = (kq * 4 + ((lane >> 2) & 3)) * WROW + uc * 64 + (lane & 3) * 16;
    vw[n] = 2 * uc < nblk ? v : OOB;
  }
  asm volatile("" ::"s"(x), "s"(wg), "s"(wu), "s"(ws), "s"(kin), "s"(vin), "s"(sk), "s"(hk), "s"(sD), "s"(wd), "s"(out));
  const rsrc_t rxt = mkrs(x, nblk ? S * D * 2 : 0), rg = mkrs(wg, D * WROW), ru = mkrs(wu, D * WROW);
  auto stage = [&](int s) AI {
    LAS char* dst = L + (s % 3) * SLOT;
    const unsigned sw = s * 64 * WROW + ft * 192;
#pragma unroll
    for (int i = 0; i < 16; ++i) dma(rxt, dst + i * 1024, vx[i & 1], (mt * 128 + 8 * i) * 1024 + s * 128);
#pragma unroll
    for (int n = 0; n < 12; ++n) dma(rg, dst + L_XW + n * 1024, vw[n % 3], sw + (n / 3) * 16 * WROW);
#pragma unroll
    for (int n = 0; n < 12; ++n) dma(ru, dst + L_UP + n * 1024, vw[n % 3] ^ 32, sw + (n / 3) * 16 * WROW);
  };
  if (w == 7 && lane < CODEPF) {
    unsigned long long pc;
    asm volatile("s_getpc_b64 %0" : "=s"(pc));
    __builtin_amdgcn_global_load_lds((const GAS unsigned*)((pc & ~127ull) + (unsigned)(j * CODEPF + lane) * 128u), (LAS unsigned*)(L + L_UR0), 4, 0, 0);
  }
  if (w == 0 && lane < 28) lds_set(L + L_CTL + 4 + 4 * lane, 0u);
  BAR();
  STAMP(1);
  v4u skv;
  if (w < 4) {
    const rsrc_t rk = mkrs(kin, S * D * 2), rv = mkrs(vin, S * D * 2);
    const int r8 = lane >> 3, p8 = lane & 7;
#pragma unroll
    for (int q = 0; q < 2; ++q) {
      const int i = 2 * w + q, row = 64 * j - 3 + 8 * i + r8;
      dma(rk, L + L_KS + i * 1024, row >= 0 ? row * 1024 + 128 * xcd + ((p8 ^ i) << 4) : OOB, 0);
      dma(rv, L + L_VS + i * 1024, r8 * 1024 + ((p8 ^ i) << 4), (64 * j + 8 * i) * 1024 + 128 * xcd);
    }
    if (w == 0) dma(rk, L + L_KS + 8192, r8 < 3 ? (64 * j + 61 + r8) * 1024 + 128 * xcd + (p8 << 4) : OOB, 0);
    skv = *(const v4u*)(sk + (64 * xcd + 16 * w + 2 * r8) * 4);
  }
  if (w == 4) { stage(0); lds_set(L + L_ORD, 1u); }
  const int m2 = (xcd & 1) * 16 + (xcd >> 1) * 4 + (j & 3), nt = j >> 2;
  const rsrc_t rws = mkrs(ws, WS_END);
  const int h = lane >> 5;
  const int k0 = w < 7 ? 11 * w : 76, nk = w < 6 ? 11 : 10;
  v4u wdf[11][2];
  unsigned T;

  if (w < 4) {
    const int wm = w & 1, wn = w >> 1;
    f32x16 acc[2][3];
#pragma unroll
    for (int t = 0; t < 2; ++t)
#pragma unroll
      for (int i = 0; i < 3; ++i)
#pragma unroll
        for (int r = 0; r < 16; ++r) acc[t][i][r] = 0.f;
#pragma unroll
    for (int t = 0; t < 2; ++t)
#pragma unroll
      for (int i = 0; i < 3; ++i) asm volatile("" : "+v"(acc[t][i]));
    const int tok = 64 * wm + (lane & 31);
    const unsigned xa0 = tok * 128 + ((h ^ ((tok >> 1) & 7)) << 4);
    unsigned wb[3];
    {
      const int mat = (lane >> 4) & 1, q = (lane & 15) >> 2, p = lane & 3;
#pragma unroll
      for (int i = 0; i < 3; ++i) {
        const int fb = 3 * wn + i;
        wb[i] = L_XW + mat * 12288 + h * 1536 + q * 64 + p * 8 + (fb >> 1) * 256 + (((fb & 1) ^ mat) << 5);
      }
    }
    bf16x8 xf[2][2], wf[2][3];
    auto rdw = [&](unsigned so, int ks, int i) AI -> bf16x8 {
      LAS char* wp = L + (wb[i] + so) + ks * 3072;
      s16x4 lo = __builtin_amdgcn_ds_read_tr16_b64_v4i16((LAS s16x4*)(wp));
      s16x4 hi = __builtin_amdgcn_ds_read_tr16_b64_v4i16((LAS s16x4*)(wp + 768));
      return __builtin_shufflevector(lo, hi, 0, 1, 2, 3, 4, 5, 6, 7);
    };
    auto ldfr = [&](unsigned so, int ks, int buf) AI {
      LAS char* xp = L + ((xa0 + so) ^ (ks << 5));
      xf[buf][0] = *(LAS bf16x8*)(xp);
      wf[buf][0] = rdw(so, ks, 0);
      xf[buf][1] = *(LAS bf16x8*)(xp + 4096);
      wf[buf][1] = rdw(so, ks, 1);
      wf[buf][2] = rdw(so, ks, 2);
    };
    auto mm = [&](int buf, int lo, int hi) AI {
#pragma unroll
      for (int e = lo; e < hi; ++e) acc[e & 1][e >> 1] = mfma32(wf[buf][e >> 1], xf[buf][e & 1], acc[e & 1][e >> 1]);
    };
    {
      const int c = lane >> 3, sg = lane & 7;
      VMW(0);
      lds_add(L + L_STG, lane);
      STAMP(10);
      wait_word(L + L_STG, 4);
      unsigned kl[11], vl[8];
      LAS char* kp = L + L_KS + sg * 1024 + (c & 3) * 4;
      const int ch = 2 * w + (c >> 2);
#pragma unroll
      for (int i = 0; i < 11; ++i) kl[i] = *(LAS unsigned*)(kp + i * 128 + ((ch ^ ((sg + (i >> 3)) & 7)) << 4));
#pragma unroll
      for (int i = 0; i < 8; ++i) vl[i] = *(LAS unsigned*)(kp + 9216 + i * 128 + ((ch ^ sg) << 4));
#pragma unroll
      for (int e = 0; e < 2; ++e) {
        const float w0 = bflo(skv[2 * e]), w1 = bfhi(skv[2 * e]), w2 = bflo(skv[2 * e + 1]), w3 = bfhi(skv[2 * e + 1]);
        float kf[11], r[8];
#pragma unroll
        for (int i = 0; i < 11; ++i) kf[i] = e ? bfhi(kl[i]) : bflo(kl[i]);
#pragma unroll
        for (int i = 0; i < 8; ++i)
          r[i] = __builtin_fmaf(kf[i + 3], w0, __builtin_fmaf(kf[i + 2], w1, __builtin_fmaf(kf[i + 1], w2, kf[i] * w3))) * (e ? bfhi(vl[i]) : bflo(vl[i]));
        *(LAS v4u*)(L + L_KO + (16 * w + 2 * c + e) * 128 + (7 - sg) * 16) = v4u{pk2(r[7], r[6]), pk2(r[5], r[4]), pk2(r[3], r[2]), pk2(r[1], r[0])};
      }
      lds_add(L + L_KVD, lane);
    }
    STAMP(19);
    __builtin_amdgcn_s_setprio(2);
    if (nblk == 0) lds_set(L + L_FREE + 4 * w, 8u);
    wait_word(L + L_FULL, 1);
    STAMP(2);
    unsigned so = 0;
    ldfr(0, 0, 0);
#define STEP(ks, nb, cb) ldfr(so, ks, nb); mm(cb, 0, 6); SGB(8, 1); SGB(0x100, 3); SGB(8, 1); SGB(0x100, 1); SGB(8, 1); SGB(0x100, 2); SGB(8, 1); SGB(0x100, 2); SGB(8, 2); SCHED();
#pragma unroll 1
    for (int s = nblk ? 0 : 8; s < 8; ++s) {
      SCHED();
      STEP(1, 1, 0) STEP(2, 0, 1)
      const unsigned fl = *(volatile LAS unsigned*)(L + (s < 7 ? L_FULL + 4 * (s + 1) : L_EPI));
      SCHED();
      STEP(3, 1, 0)
      lds_set(L + L_FREE + 4 * w, s + 1);
      so = so == 2 * SLOT ? 0 : so + SLOT;
      mm(1, 0, 1);
      SCHED();
      STAMP(11 + s);
      if (s < 7 && __builtin_amdgcn_readfirstlane(fl) == 0) wait_word(L + L_FULL + 4 * (s + 1), 1);
      asm volatile("" ::: "memory");
      ldfr(so, 0, 0);
      mm(1, 1, 6);
      SGB(0x100, 3); SGB(8, 1); SGB(0x100, 1); SGB(8, 1); SGB(0x100, 2); SGB(8, 1); SGB(0x100, 2); SGB(8, 2);
      SCHED();
    }
    wait_word(L + L_EPI, 1);
    wait_word(L + L_POK, 2);
    STAMP(3);
    T = lds_get(L + L_CTL);
    {
      const int mt64 = 2 * mt + wm;
      f32x2 nm[2], K1[2], K2[2], iR2[2];
#pragma unroll
      for (int t = 0; t < 2; ++t) {
        const f32x2 st = *(LAS f32x2*)(L + L_ST + (tok + 32 * t) * 8);
        const float r = st[1], k1 = -2.3022082f * r, k2 = (-2.3022082f * 0.044715f) * r * r * r, ir = __builtin_amdgcn_rcpf(r * r);
        nm[t] = f32x2{-st[0], -st[0]}; K1[t] = f32x2{k1, k1}; K2[t] = f32x2{k2, k2}; iR2[t] = f32x2{ir, ir};
      }
#pragma unroll
      for (int i = 0; i < 3; ++i) {
        LAS char* cp = L + L_CS + (16 * (3 * wn + i) + 4 * h) * 4;
        const f32x4 cg0 = *(LAS f32x4*)(cp), cg1 = *(LAS f32x4*)(cp + 32), cu0 = *(LAS f32x4*)(cp + 384), cu1 = *(LAS f32x4*)(cp + 416);
        const unsigned vo = 3 * wn + i < nblk ? lane * 16 : OOB;
#pragma unroll
        for (int t = 0; t < 2; ++t) {
          v4u o;
#pragma unroll
          for (int e = 0; e < 4; ++e) {
            const f32x4 cgv = e < 2 ? cg0 : cg1, cuv = e < 2 ? cu0 : cu1;
            const f32x2 cg = {cgv[2 * (e & 1)], cgv[2 * (e & 1) + 1]}, cu = {cuv[2 * (e & 1)], cuv[2 * (e & 1) + 1]};
            const f32x2 ag = {acc[t][i][2 * e], acc[t][i][2 * e + 1]}, au = {acc[t][i][8 + 2 * e], acc[t][i][9 + 2 * e]};
            const f32x2 G = __builtin_elementwise_fma(nm[t], cg, ag), U = __builtin_elementwise_fma(nm[t], cu, au);
            const f32x2 ww = G * __builtin_elementwise_fma(G * G, K2[t], K1[t]);
            const f32x2 ex = {__builtin_amdgcn_exp2f(ww[0]), __builtin_amdgcn_exp2f(ww[1])};
            const f32x2 dn = __builtin_elementwise_fma(ex, iR2[t], iR2[t]);
            const f32x2 rc = {__builtin_amdgcn_rcpf(dn[0]), __builtin_amdgcn_rcpf(dn[1])};
            const f32x2 hv = (G * U) * rc;
            o[e] = pk2(hv[0], hv[1]);
          }
          st128(o, rws, vo, WS_HT + ((mt64 * NFB + ft * 6 + 3 * wn + i) * 2 + t) * 1024);
          {
            const int q0 = (2 * i + t) * NEPI;
#pragma unroll
            for (int qq = q0; qq < q0 + NEPI; ++qq) wdf[qq >> 1][qq & 1] = ld128(rws, lane * 16, WS_WDT + ((nt * NFB + k0) * 2 + qq) * 1024, 1);
#pragma unroll
            for (int g4 = 0; g4 < NEPI; ++g4) { SGB(0x2, 52 / (NEPI + 1)); SGB(0x20, 1); }
            SCHED();
          }
        }
      }
      lds_set(L + L_FREE + 4 * w, 9u);
      STAMP(4);
      VMW(0);
      flag_set(rws, WS_HF + (mt64 * 32 + ft * 2 + wn) * FL, T, lane, 3 * wn < nblk);
    }
    __builtin_amdgcn_s_setprio(0);
    STAMP(5);
#pragma unroll
    for (int qq = 6 * NEPI; qq < 22; ++qq) wdf[qq >> 1][qq & 1] = ld128(rws, lane * 16, WS_WDT + ((nt * NFB + k0) * 2 + qq) * 1024, 1);
    SCHED();
    wait_free(L, 8);
  } else {
    const int chl = (w - 4) >> 1, d0 = 64 * xcd + 2 * j, d = d0 + chl;
    const int m0 = 1 + (w & 1) * 128 + lane;
    auto load_wd = [&]() AI {
#pragma unroll
      for (int i = 0; i < 11; ++i)
#pragma unroll
        for (int e = 0; e < 2; ++e) wdf[i][e] = ld128(rws, i < nk ? lane * 16 : OOB, WS_WDT + ((nt * NFB + k0 + i) * 2 + e) * 1024, 1);
      SCHED();
    };
    if (w < 6) {
      __builtin_amdgcn_s_setprio(3);
      if (w == 5) {
        wait_word(L + L_ORD, 1);
        stage(1);
        stage(2);
        VMW(40);
        lds_set(L + L_FULL + 4, 1u);
        STAMP(12);
      }
      VMW(0);
      lds_set(L + L_FULL + 8 * (w - 4), 1u);
      STAMP(3 + 2 * w);
#pragma unroll 1
      for (int n = w - 1; n < 8; n += 2) {
        wait_free(L, n - 2);
        stage(n);
        lds_set(L + L_ISS + 4 * (n & 1), n);
        if (n >= 6) break;
        VMW(0);
        lds_set(L + L_FULL + 4 * n, 1u);
        STAMP(11 + n);
      }
      VMW(0);
      lds_set(L + L_FULL + 4 * (11 - w), 1u);
      STAMP(17);
      __builtin_amdgcn_s_setprio(0);
    } else {
      const int hw = w - 6;
      unsigned E1 = 0;
      if (hw) E1 = flag_get(ws, WS_EPOCH) + 1u;
      v4u pl[16];
      if (HWAIT) wait_word(L + L_ORD, 1);
      if (hw) {
        const rsrc_t rx = mkrs(x, S * D * 2);
#pragma unroll
        for (int i = 0; i < 8; ++i) pl[i] = ld128(rx, (lane >> 3) * 1024 + (lane & 7) * 16, (mt * 128 + ft * 8) * 1024 + i * 128, 0);
        const rsrc_t rw = mkrs(wd, F * D * 2);
#pragma unroll
        for (int u = 0; u < 3; ++u) {
          const int uid = b + 256 * u;
#pragma unroll
          for (int i = 0; i < 2; ++i)
            dma(rw, L + L_WS + (2 * u + i) * 1024, uid < 688 ? (lane >> 3) * 1024 + (lane & 7) * 16 : OOB, (16 * (uid >> 3) + 8 * i) * 1024 + (uid & 7) * 128);
        }
        SCHED();
        lds_set(L + L_CTL, E1);
        lds_set(L + L_KCT, 1u);
      } else {
        const int c = lane >> 1, cc = c < 12 ? c : c - 12;
        const bool ok = lane < 48 && cc < 2 * nblk;
        const char* base = (const char*)(c < 12 ? wg : wu) + (ok ? (size_t)((32 * mt + 16 * (lane & 1)) * WROW + ft * 192 + cc * 16) : 0);
#pragma unroll
        for (int i = 0; i < 16; ++i) pl[i] = *(const v4u*)(base + i * WROW);
        SCHED();
      }
      __builtin_amdgcn_s_setprio(HPRIO);
      wait_word(L + L_KCT, 1);
      E1 = lds_get(L + L_CTL);
      if (hw) {
        float s0 = 0.f, s1 = 0.f;
#pragma unroll
        for (int i = 0; i < 8; ++i)
#pragma unroll
          for (int e = 0; e < 4; ++e) { s0 = sadd(s0, bflo(pl[i][e])); s1 = sadd(s1, bfhi(pl[i][e])); }
        const float mean = sum8(s0 + s1) * (1.0f / D);
        float q0 = 0.f, q1 = 0.f;
#pragma unroll
        for (int i = 0; i < 8; ++i)
#pragma unroll
          for (int e = 0; e < 4; ++e) {
            const float a0 = ssub(bflo(pl[i][e]), mean), a1 = ssub(bfhi(pl[i][e]), mean);
            q0 = sfma(a0, a0, q0); q1 = sfma(a1, a1, q1);
          }
        const float sd = rbf(__builtin_sqrtf(rbf(rbf(sum8(q0 + q1) * (1.0f / D)) + 1e-5f)));
        st64(v2u{f2u(rbf(mean)), f2u(1.0f / sd)}, rws, (lane & 7) == 0 ? WS_STATS + (mt * 128 + ft * 8 + (lane >> 3)) * 8 : OOB, 0);
        LAS char* tp = L + L_WS + (4 * h + ((lane & 15) >> 2)) * 128 + (4 * ((lane >> 4) & 1) + (lane & 3)) * 16;
        VMW(1);
#pragma unroll
        for (int q = 0; q < 6; ++q) {
          const int uid = b + 256 * (q >> 1);
          const v2u lo = __builtin_bit_cast(v2u, __builtin_amdgcn_ds_read_tr16_b64_v4i16((LAS s16x4*)(tp + (q >> 1) * 2048 + (q & 1) * 8)));
          const v2u hi = __builtin_bit_cast(v2u, __builtin_amdgcn_ds_read_tr16_b64_v4i16((LAS s16x4*)(tp + (q >> 1) * 2048 + (q & 1) * 8 + 1024)));
          st128(v4u{lo.x, lo.y, hi.x, hi.y}, rws, uid < 688 ? lane * 16 : OOB, WS_WDT + (((uid & 7) * NFB + (uid >> 3)) * 2 + (q & 1)) * 1024);
        }
      } else {
        float cs[8];
#pragma unroll
        for (int e = 0; e < 8; ++e) cs[e] = 0.f;
#pragma unroll
        for (int i = 0; i < 16; ++i)
#pragma unroll
          for (int e = 0; e < 4; ++e) { cs[2 * e] = sadd(cs[2 * e], bflo(pl[i][e])); cs[2 * e + 1] = sadd(cs[2 * e + 1], bfhi(pl[i][e])); }
        float o[4];
#pragma unroll
        for (int e = 0; e < 4; ++e) {
          const float mine = (lane & 1) ? cs[4 + e] : cs[e], give = (lane & 1) ? cs[e] : cs[4 + e];
          o[e] = mine + dpp<0xB1>(give);
        }
        st128(v4u{f2u(o[0]), f2u(o[1]), f2u(o[2]), f2u(o[3])}, rws, (lane < 48 && nblk) ? lane * 16 : OOB, WS_CSP + (ft * 16 + mt) * 768);
      }
      if (!hw) {
        wait_word(L + L_KVD, 4);
#pragma unroll
        for (int i = 0; i < 8; ++i)
          st128(*(LAS v4u*)(L + L_KO + i * 1024 + lane * 16), rws, WS_KVR + (64 * xcd + (lane >> 3)) * 4096 + (1984 - 64 * j) * 2 + (lane & 7) * 16, i * 8 * 4096);
      }
      STAMP(12);
      __builtin_amdgcn_s_setprio(0);
      v4u hl[2][2][2];
#pragma unroll
      for (int c = 0; c < 2; ++c) {
        const rsrc_t rh = mkrs(hk + (d0 + c) * S, S * 2);
#pragma unroll
        for (int i = 0; i < 2; ++i) { hl[c][i][0] = ld128(rh, (m0 + 64 * i - 2) * 16, 0, 0); hl[c][i][1] = ld128(rh, (m0 + 64 * i - 1) * 16, 0, 0); }
      }
      SCHED();
      arrive(L, L_PCT, 2, rws, WS_PF + b * FL, E1, lane);
      STAMP(13);
      wait_word(L + L_PCT, 2);
      build_hc(L + L_HC0, m0, hl[0][0][0], hl[0][0][1]);
      build_hc(L + L_HC0, m0 + 64, hl[0][1][0], hl[0][1][1]);
      STAMP(14);
      {
        const unsigned po = WS_PF + (hw ? (lane < 16 ? wg_of_tile(mt, lane) : wg_of_tile(lane & 15, ft)) : (lane & 31) * 8 + (lane < 32 ? xcd : nt)) * FL;
        bool ok = false;
#pragma unroll 1
        for (int n = POLLST; n < 8 && !ok; ++n) {
          wait_word(L + L_ISS + 4 * (n & 1), n);
          ok = all_of(flag_get(ws, po) == E1);
        }
        if (!ok) poll_words<PSLEEP>(ws, po, E1);
        ORDER_BEHIND_POLL();
      }
      STAMP(15);
      if (hw) {
#pragma unroll
        for (int i = 0; i < 16; ++i) pl[i] = ld128(rws, (lane < 48 && nblk) ? lane * 16 : OOB, WS_CSP + (ft * 16 + i) * 768, 1);
        const v4u sv = ld128(rws, lane * 16, WS_STATS + mt * 1024, 1);
        SCHED();
        lds_add(L + L_POK, lane);
        f32x4 cs = {0.f, 0.f, 0.f, 0.f};
#pragma unroll
        for (int i = 0; i < 16; ++i)
#pragma unroll
          for (int e = 0; e < 4; ++e) cs[e] = sadd(cs[e], u2f(pl[i][e]));
        if (lane < 48) *(LAS f32x4*)(L + L_CS + lane * 16) = cs;
        *(LAS v4u*)(L + L_ST + lane * 16) = sv;
        lds_set(L + L_EPI, 1u);
      } else {
#pragma unroll
        for (int i = 0; i < 8; ++i) pl[i] = ld128(rws, lane * 16, WS_KVR + d0 * 4096 + i * 1024, 1);
        SCHED();
        lds_add(L + L_POK, lane);
#pragma unroll
        for (int i = 0; i < 4; ++i) *(LAS v4u*)(L + L_UR0 + i * 1024 + lane * 16) = pl[i];
      }
      STAMP(16);
      wait_free(L, 6);
      build_hc(L + L_HC1, m0, hl[1][0][0], hl[1][0][1]);
      build_hc(L + L_HC1, m0 + 64, hl[1][1][0], hl[1][1][1]);
      {
        LAS char* hz = L + (hw ? L_HC1 : L_HC0);
        LAS char* uz = L + (hw ? L_UR1 : L_UR0);
        if (lane < 8) *(LAS v4u*)(hz + lane * HC_P) = v4u{0, 0, 0, 0};
        else if (lane < 40) *(LAS v4u*)(uz + 4096 + (lane - 8) * 16) = v4u{0, 0, 0, 0};
        if (!hw) {
#pragma unroll
          for (int i = 0; i < 4; ++i) *(LAS v4u*)(L + L_UR1 + i * 1024 + lane * 16) = pl[4 + i];
        }
      }
      lds_add(L + L_CVK, lane);
    }
    LAS char* hc = L + (chl ? L_HC1 : L_HC0);
    LAS char* ur = L + (chl ? L_UR1 : L_UR0);
    const float Dd = bflo(sD[d]);
    STAMP(18);
    wait_word(L + L_POK, 2);
    wait_word(L + L_ISS + 4, 7);
    load_wd();
    STAMP(2);
    wait_word(L + L_CVK, 2);
    wait_free(L, CONVGO);
    STAMP(3);
    T = lds_get(L + L_CTL);
    __builtin_amdgcn_s_setprio(2);
    if (w & 1) conv_part<6, 8>(hc, ur, lane, Dd, rws, d);
    else conv_part<0, 6>(hc, ur, lane, Dd, rws, d);
    __builtin_amdgcn_s_setprio(0);
    STAMP(4);
    arrive(L, L_ZCT, 4, rws, WS_ZF + (d0 >> 1) * FL, T, lane);
    STAMP(5);
  }

  {
    const int tb = w >> 2, q = w & 3;
    const int tl = 32 * tb + (lane & 31), tok = 64 * m2 + tl, dd = 64 * nt + 16 * q + 8 * h;
    const v4u xv = *(const v4u*)(x + tok * D + dd);
    poll_words(ws, WS_HF + (m2 * 32 + umin(k0 / 3 + (lane & 7), (k0 + nk - 1) / 3)) * FL, T);
    STAMP(6);
    const v2u stv2 = __builtin_amdgcn_raw_buffer_load_b64(rws, WS_STATS + tok * 8, 0, SC1);
    v4u hf[11][2];
#pragma unroll
    for (int i = 0; i < 8; ++i)
#pragma unroll
      for (int t = 0; t < 2; ++t) hf[i][t] = ld128(rws, lane * 16, WS_HT + ((m2 * NFB + k0 + i) * 2 + t) * 1024, 1);
    SCHED();
    const unsigned zfo = WS_ZF + (32 * nt + 8 * q + (lane & 7)) * FL;
    const unsigned zf = flag_get(ws, zfo);
    SCHED();
#pragma unroll
    for (int i = 8; i < 11; ++i)
#pragma unroll
      for (int t = 0; t < 2; ++t) hf[i][t] = ld128(rws, i < nk ? lane * 16 : OOB, WS_HT + ((m2 * NFB + k0 + i) * 2 + t) * 1024, 1);
    SCHED();
    f32x16 acc[2][2];
#pragma unroll
    for (int t = 0; t < 2; ++t)
#pragma unroll
      for (int e = 0; e < 2; ++e)
#pragma unroll
        for (int r = 0; r < 16; ++r) acc[t][e][r] = 0.f;
#pragma unroll
    for (int i = 0; i < 8; ++i)
#pragma unroll
      for (int t = 0; t < 2; ++t)
#pragma unroll
        for (int e = 0; e < 2; ++e) acc[t][e] = mfma32(asb(wdf[i][e]), asb(hf[i][t]), acc[t][e]);
    SCHED();
    if (!all_of(zf == T)) poll_words(ws, zfo, T);
    ORDER_BEHIND_POLL();
    const v4u zv = ld128(rws, WS_ZT + (64 * nt + 16 * q + (lane >> 2)) * 4096 + (64 * m2 + 32 * tb + 8 * (lane & 3)) * 2, 0, 1);
    SCHED();
#pragma unroll
    for (int i = 8; i < 11; ++i)
#pragma unroll
      for (int t = 0; t < 2; ++t)
#pragma unroll
        for (int e = 0; e < 2; ++e) acc[t][e] = mfma32(asb(wdf[i][e]), asb(hf[i][t]), acc[t][e]);
    STAMP(7);
#pragma unroll
    for (int t = 0; t < 2; ++t)
#pragma unroll
      for (int qq = 0; qq < 4; ++qq) {
        const v4u o = {pk2(acc[t][0][4 * qq], acc[t][0][4 * qq + 1]), pk2(acc[t][0][4 * qq + 2], acc[t][0][4 * qq + 3]),
                       pk2(acc[t][1][4 * qq], acc[t][1][4 * qq + 1]), pk2(acc[t][1][4 * qq + 2], acc[t][1][4 * qq + 3])};
        *(LAS v4u*)(L + L_RED + w * 8192 + (32 * t + (lane & 31)) * 128 + (((2 * qq + h) ^ (lane & 7)) << 4)) = o;
      }
    {
      *(LAS v4u*)(L + L_ZS + w * 1024 + lane * 16) = zv;
      LAS char* zp = L + L_ZS + w * 1024 + (8 * h + ((lane & 15) >> 2)) * 64 + (16 * ((lane >> 4) & 1) + 4 * (lane & 3)) * 2;
      const v2u z0 = __builtin_bit_cast(v2u, __builtin_amdgcn_ds_read_tr16_b64_v4i16((LAS s16x4*)(zp)));
      const v2u z1 = __builtin_bit_cast(v2u, __builtin_amdgcn_ds_read_tr16_b64_v4i16((LAS s16x4*)(zp + 256)));
      const unsigned zz[4] = {z0.x, z0.y, z1.x, z1.y};
      const float m = u2f(stv2.x), r = u2f(stv2.y);
      v4u o;
#pragma unroll
      for (int e = 0; e < 4; ++e) {
        const float x0 = bflo(xv[e]), x1 = bfhi(xv[e]);
        const unsigned c = pk2(ssub(x0, m), ssub(x1, m));
        const unsigned xn = pk2(smul(bflo(c), r), smul(bfhi(c), r));
        const unsigned a = pk2(smul(bflo(xn), bflo(zz[e])), smul(bfhi(xn), bfhi(zz[e])));
        o[e] = pk2(sadd(x0, bflo(a)), sadd(x1, bfhi(a)));
      }
      *(LAS v4u*)(L + L_TP + tl * 128 + (((2 * q + h) ^ (lane & 7)) << 4)) = o;
    }
    BAR();
    STAMP(8);
    {
      LAS char* rp = L + L_RED + w * 1024 + (lane >> 3) * 128 + (((lane & 7) ^ (lane >> 3)) << 4);
      f32x2 f[4];
#pragma unroll
      for (int e = 0; e < 4; ++e) f[e] = f32x2{0.f, 0.f};
#pragma unroll
      for (int ww = 0; ww < 8; ++ww) {
        const v4u p = *(LAS v4u*)(rp + ww * 8192);
#pragma unroll
        for (int e = 0; e < 4; ++e) f[e] += f32x2{bflo(p[e]), bfhi(p[e])};
      }
      const v4u tp = *(LAS v4u*)(rp + L_TP);
      v4u o;
#pragma unroll
      for (int e = 0; e < 4; ++e) {
        const unsigned ff = pk2(f[e][0], f[e][1]);
        o[e] = pk2(bflo(tp[e]) + bflo(ff), bfhi(tp[e]) + bfhi(ff));
      }
      __builtin_amdgcn_raw_buffer_store_b128(o, mkrs(out, S * D * 2), ((64 * m2 + 8 * w + (lane >> 3)) * D + 64 * nt + 8 * (lane & 7)) * 2, 0, OUTAUX);
    }
    STAMP(9);
    if (b == 0 && w == 7) {
#pragma unroll 1
      for (int i = 0; i < 2; ++i) poll_words2(ws, WS_PF + (128 * i + lane) * FL, WS_PF + (128 * i + 64 + lane) * FL, T);
      if (lane == 0) word_set(ws, WS_EPOCH, T);
    }
  }
}

#define WSEND 0
#define WSOFF (WSEND ? ((ws_size - (16u << 20)) & ~(size_t)4095) : 0)
extern "C" void kernel_launch(void* const* d_in, const int* in_sizes, int n_in, void* d_out, int out_size, void* d_ws, size_t ws_size,
                              hipStream_t stream) {
  fused_kernel<<<256, 512, 0, stream>>>((const u16*)d_in[0], (const u16*)d_in[7], (const u16*)d_in[8], (char*)d_ws + WSOFF, (const u16*)d_in[1],
                                        (const u16*)d_in[2], (const u16*)d_in[3], (const u16*)d_in[4], (const u16*)d_in[5], (const u16*)d_in[9],
                                        (u16*)d_out);
}
```

```cpp
#include <hip/hip_runtime.h>
#include <hip/hip_bf16.h>
#include <stdint.h>

typedef unsigned short u16;
typedef unsigned v4u __attribute__((ext_vector_type(4)));
typedef unsigned v2u __attribute__((ext_vector_type(2)));
typedef short bf16x8 __attribute__((ext_vector_type(8)));
typedef short s16x4 __attribute__((ext_vector_type(4)));
typedef float f32x16 __attribute__((ext_vector_type(16)));
typedef float f32x4 __attribute__((ext_vector_type(4)));
typedef float f32x2 __attribute__((ext_vector_type(2)));
typedef __bf16 bf2 __attribute__((ext_vector_type(2)));
#define LAS __attribute__((address_space(3)))
#define GAS __attribute__((address_space(1)))
#define DI __device__ __forceinline__
#define AI __attribute__((always_inline))
typedef __amdgpu_buffer_rsrc_t rsrc_t;

constexpr int S = 2048, D = 512, F = 1376, NFB = 86;
constexpr int WROW = F * 2;
constexpr unsigned FL = 128;
constexpr unsigned WS_EPOCH = 0, WS_PF = 1024, WS_ZF = WS_PF + 256 * FL, WS_HF = WS_ZF + 256 * FL;
constexpr unsigned WS_STATS = WS_HF + 1024 * FL;
constexpr unsigned WS_CSP = WS_STATS + 16384;
constexpr unsigned WS_KVR = 524288;
static_assert(WS_CSP + 15 * 16 * 768 <= WS_KVR, "ws");
constexpr unsigned WS_ZT = WS_KVR + 2097152;
constexpr unsigned WS_WDT = WS_ZT + 2097152;
constexpr unsigned WS_HT = WS_WDT + 8 * 86 * 2048;
constexpr unsigned WS_END = WS_HT + 32 * 86 * 2048;
constexpr int SLOT = 40960, L_XW = 16384, L_UP = 28672;
constexpr int L_CS = 122880, L_ST = L_CS + 768, L_CTL = L_CS + 1792;
constexpr int L_ZCT = L_CTL + 4, L_CVK = L_CTL + 8, L_EPI = L_CTL + 12, L_FREE = L_CTL + 16, L_FULL = L_CTL + 32, L_POK = L_CTL + 64, L_ISS = L_CTL + 88,
              L_STG = L_CTL + 72, L_PCT = L_CTL + 76, L_KCT = L_CTL + 80, L_ORD = L_CTL + 84, L_KVD = L_CTL + 96, L_XFL = L_CTL + 100;
constexpr int HC_P = 4160, HC_B = 8 * HC_P;
constexpr int L_HC0 = 124928, L_UR0 = L_HC0 + HC_B, L_HC1 = 2 * SLOT, L_UR1 = L_HC1 + HC_B;
constexpr int L_KS = L_HC0, L_VS = L_KS + 9216, L_WS = L_VS + 8192, L_KO = L_WS + 6144;
constexpr int L_XCH = L_UR1 + 4608;
static_assert(L_XCH + 2048 <= 3 * SLOT, "lds");
constexpr int L_RED = 0, L_TP = 65536, L_ZS = 73728;
constexpr int LDS_BYTES = 163840;
static_assert(L_UR0 + 4608 <= LDS_BYTES && L_UR1 + 4608 <= 3 * SLOT, "lds");
constexpr int SC1 = 16;
constexpr unsigned OOB = 0x80000000u;
constexpr unsigned SPIN_CAP = 1u << 18, LDS_CAP = 1u << 22;
constexpr int KA = 12;

#define BAR() asm volatile("s_waitcnt lgkmcnt(0)\n\ts_barrier" ::: "memory")
#define SGB(m, n) __builtin_amdgcn_sched_group_barrier(m, n, 0)
#define SCHED() __builtin_amdgcn_sched_barrier(0)
#define VMW(n) asm volatile("s_waitcnt vmcnt(%0)" ::"i"(n) : "memory")
#define RLX_AGENT __ATOMIC_RELAXED, __HIP_MEMORY_SCOPE_AGENT

DI float u2f(unsigned u) { return __builtin_bit_cast(float, u); }
DI unsigned f2u(float f) { return __builtin_bit_cast(unsigned, f); }
DI bool all_of(bool c) { return __builtin_amdgcn_ballot_w64(!c) == 0; }
template <int CTRL> DI float dpp(float v) { return u2f(__builtin_amdgcn_update_dpp(0u, f2u(v), CTRL, 0xf, 0xf, true)); }
DI float sum8(float v) { v += dpp<0xB1>(v); v += dpp<0x4E>(v); v += dpp<0x141>(v); return v; }
DI unsigned umin(unsigned a, unsigned b) { return a < b ? a : b; }
DI float sadd(float a, float b) { float d; asm("v_add_f32 %0, %1, %2" : "=v"(d) : "v"(a), "v"(b)); return d; }
DI float ssub(float a, float b) { float d; asm("v_sub_f32 %0, %1, %2" : "=v"(d) : "v"(a), "v"(b)); return d; }
DI float smul(float a, float b) { float d; asm("v_mul_f32 %0, %1, %2" : "=v"(d) : "v"(a), "v"(b)); return d; }
DI float sfma(float a, float b, float c) { float d; asm("v_fma_f32 %0, %1, %2, %3" : "=v"(d) : "v"(a), "v"(b), "v"(c)); return d; }
DI float bflo(unsigned u) { return u2f(u << 16); }
DI float bfhi(unsigned u) { return u2f(u & 0xffff0000u); }
DI unsigned pk2(float a, float b) { f32x2 v = {a, b}; return __builtin_bit_cast(unsigned, __builtin_convertvector(v, bf2)); }
DI float rbf(float a) { return bflo(pk2(a, a)); }
DI rsrc_t mkrs(const void* p, unsigned n) { return __builtin_amdgcn_make_buffer_rsrc((void*)p, 0, n, 0x00020000); }
DI v4u ld128(rsrc_t r, unsigned vo, unsigned so, int aux) { return aux ? __builtin_amdgcn_raw_buffer_load_b128(r, vo, so, SC1) : __builtin_amdgcn_raw_buffer_load_b128(r, vo, so, 0); }
DI void st128(v4u v, rsrc_t r, unsigned vo, unsigned so) { __builtin_amdgcn_raw_buffer_store_b128(v, r, vo + so, 0, SC1); }
DI void st64(v2u v, rsrc_t r, unsigned vo, unsigned so) { __builtin_amdgcn_raw_buffer_store_b64(v, r, vo, so, SC1); }
DI void dma(rsrc_t r, LAS char* dst, unsigned vo, unsigned so) { __builtin_amdgcn_raw_ptr_buffer_load_lds(r, (LAS void*)dst, 16, vo, so, 0, 0); }
DI bf16x8 asb(v4u v) { return __builtin_bit_cast(bf16x8, v); }
DI f32x16 mfma32(bf16x8 a, bf16x8 b, f32x16 c) { return __builtin_amdgcn_mfma_f32_32x32x16_bf16(a, b, c, 0, 0, 0); }
DI f32x4 mfma16(bf16x8 a, bf16x8 b, f32x4 c) { return __builtin_amdgcn_mfma_f32_16x16x32_bf16(a, b, c, 0, 0, 0); }
DI void word_set(char* ws, unsigned off, unsigned T) { __hip_atomic_store((GAS unsigned*)(ws + off), T, RLX_AGENT); }
DI void flag_set(rsrc_t rws, unsigned off, unsigned T, int lane, bool on = true) { st128(v4u{T, T, T, T}, rws, (lane < 8 && on) ? lane * 16 : OOB, off); }
DI unsigned word_get(char* ws, unsigned off) { return __hip_atomic_load((GAS unsigned*)(ws + off), RLX_AGENT); }
DI v2u flag_get(char* ws, unsigned off) { return __builtin_bit_cast(v2u, __hip_atomic_load((GAS unsigned long long*)(ws + off), RLX_AGENT)); }
DI bool flag_up(v2u v, unsigned T) { return all_of(v.x == T && v.y == T); }
#define ORDER_BEHIND_POLL() __builtin_amdgcn_fence(__ATOMIC_ACQUIRE, "wavefront")

template <int SLEEP>
DI void poll_flags(char* ws, unsigned off, unsigned T) {
#pragma nounroll
  for (unsigned spins = 0; spins < SPIN_CAP; ++spins) {
    if (flag_up(flag_get(ws, off), T)) break;
    __builtin_amdgcn_s_sleep(SLEEP);
  }
  ORDER_BEHIND_POLL();
}
DI int wg_of_tile(int mt, int ft) {
  const int mh = mt >> 3, ml = mt & 7, idx = ft * 8 + ml;
  const int fq = ft < 14 ? idx / 28 : ml >> 1;
  const int j = ft < 14 ? idx - 28 * fq : 28 + 2 * (ft - 14) + (ml & 1);
  return j * 8 + fq * 2 + mh;
}
DI unsigned lds_get(LAS char* p) { return __builtin_amdgcn_readfirstlane(*(volatile LAS unsigned*)p); }
DI void lds_set(LAS char* p, unsigned v) { *(volatile LAS unsigned*)p = v; }
DI void wait_word(LAS char* p, unsigned want) {
#pragma nounroll
  for (unsigned spins = 0; spins < LDS_CAP && lds_get(p) < want; ++spins) __builtin_amdgcn_s_sleep(1);
  asm volatile("" ::: "memory");
}
DI void wait_free(LAS char* L, unsigned want) {
#pragma nounroll
  for (unsigned spins = 0; spins < LDS_CAP; ++spins) {
    const v4u f = *(volatile LAS v4u*)(L + L_FREE);
    const unsigned m = __builtin_amdgcn_readfirstlane(umin(umin(f.x, f.y), umin(f.z, f.w)));
    if (m >= want) break;
    __builtin_amdgcn_s_sleep(1);
  }
  asm volatile("" ::: "memory");
}
DI void lds_add(LAS char* p, int lane) {
  if (lane == 0) __hip_atomic_fetch_add((LAS unsigned*)p, 1u, __ATOMIC_RELAXED, __HIP_MEMORY_SCOPE_WORKGROUP);
}
DI void arrive(LAS char* L, int ctr, unsigned n, rsrc_t rws, unsigned flag, unsigned T, int lane) {
  VMW(0);
  unsigned old = 0;
  if (lane == 0) old = __hip_atomic_fetch_add((LAS unsigned*)(L + ctr), 1u, __ATOMIC_RELAXED, __HIP_MEMORY_SCOPE_WORKGROUP);
  if (__builtin_amdgcn_readfirstlane(old) == n - 1) flag_set(rws, flag, T, lane);
}

DI void build_hc(LAS char* hc, int m, v4u lo, v4u hi) {
  unsigned d[8] = {lo.x, lo.y, lo.z, lo.w, hi.x, hi.y, hi.z, hi.w};
  unsigned o[7];
#pragma unroll
  for (int i = 0; i < 7; ++i) o[i] = __builtin_amdgcn_alignbit(d[i + 1], d[i], 16);
#pragma unroll
  for (int c = 0; c < 8; ++c) {
    const int sg = c + 1;
    v4u v;
    if (sg & 1) { const int i = (sg - 1) / 2; v = v4u{o[i], o[i + 1], o[i + 2], o[i + 3]}; }
    else { const int i = sg / 2; v = v4u{d[i], d[i + 1], d[i + 2], d[i + 3]}; }
    *(LAS v4u*)(hc + c * HC_P + m * 16) = v;
  }
}

template <bool X>
DI v2u conv_part(LAS char* hc, LAS char* ur, LAS char* xch, LAS char* xfl, LAS char* go, int lane, float Dd, rsrc_t rws, int d, char* ws, unsigned hfo) {
  v2u hv = {0, 0};
  constexpr int NQ = X ? 8 : 6, N0 = X ? 5 : 0, NT = X ? 3 : 5;
  const int b = lane & 15, g = lane >> 4;
  LAS char* ab = hc + (b & 7) * HC_P + (g + (b >> 3)) * 16;
  LAS char* bb = ur + 2 * (2032 - 16 * b + 8 * g) - 512 * (NQ - 1);
  f32x4 acc[6];
#pragma unroll
  for (int n = 0; n < 6; ++n) acc[n] = f32x4{0.f, 0.f, 0.f, 0.f};
  bf16x8 A[2][NQ], B[2][NQ];
  v2u uu[NT];
  auto rd = [&](int r, int c) AI {
    const int nq = (X || r < 4) ? NQ : NQ - 1;
#pragma unroll
    for (int q = 0; q < NQ; ++q)
      if (q < nq) {
        A[c][q] = *(LAS bf16x8*)(ab + 512 * q + 64 * r);
        B[c][X ? NQ - 1 - q : q] = *(LAS bf16x8*)(bb + 512 * (X ? q : NQ - 1 - q) + 64 * r);
      }
  };
  rd(0, 0);
  SCHED();
  wait_word(go, 9);
  __builtin_amdgcn_s_setprio(2);
  SCHED();
#pragma unroll
  for (int r = 0; r < 8; ++r) {
    const int c = r & 1;
    if (r == 6) hv = flag_get(ws, hfo);
    if (r < 7) rd(r + 1, c ^ 1);
    else {
#pragma unroll
      for (int n = 0; n < NT; ++n) uu[n] = *(LAS v2u*)(ur + 2 * (2044 - (256 * (N0 + n) + 16 * b + 4 * g)));
    }
    if (X) {
#pragma unroll
      for (int q = 0; q < NQ; ++q) {
        acc[4 + (q & 1)] = mfma16(A[c][q], B[c][7 - q], acc[4 + (q & 1)]);
        if (q >= 1) acc[2 + (q & 1)] = mfma16(A[c][q - 1], B[c][7 - q], acc[2 + (q & 1)]);
        if (q >= 2 && r >= 4) acc[q & 1] = mfma16(A[c][q - 2], B[c][7 - q], acc[q & 1]);
      }
    } else {
      const int nt = r < 4 ? 6 : 5;
#pragma unroll
      for (int q = 0; q < NQ; ++q)
#pragma unroll
        for (int p = 0; p < NQ; ++p)
          if (q + p < nt) acc[q + p] = mfma16(A[c][q], B[c][p], acc[q + p]);
    }
#pragma unroll
    for (int g2 = 0; g2 < 16; ++g2) { SGB(0x100, 1); SGB(8, 1); }
    SGB(8, 8);
    SCHED();
    if (!X && r == 4) { *(LAS f32x4*)(xch + lane * 16) = acc[5]; lds_set(xfl, 1u); SCHED(); }
  }
  f32x4 y[NT];
  if (X) {
    wait_word(xfl, 1);
    y[2] = acc[4] + acc[5]; y[1] = acc[2] + acc[3]; y[0] = acc[0] + acc[1] + *(LAS f32x4*)(xch + lane * 16);
  } else {
#pragma unroll
    for (int n = 0; n < NT; ++n) y[n] = acc[n];
  }
#pragma unroll
  for (int n = 0; n < NT; ++n) {
    v2u o;
    o.x = pk2(__builtin_fmaf(bfhi(uu[n].y), Dd, y[n][0]), __builtin_fmaf(bflo(uu[n].y), Dd, y[n][1]));
    o.y = pk2(__builtin_fmaf(bfhi(uu[n].x), Dd, y[n][2]), __builtin_fmaf(bflo(uu[n].x), Dd, y[n][3]));
    st64(o, rws, WS_ZT + d * 4096 + (16 * b + 4 * g) * 2 + 512 * (N0 + n), 0);
  }
  return hv;
}

template <int NK, int NW, bool TP>
DI void gemm2_part(LAS char* L, char* ws, rsrc_t rws, v4u (&wdf)[NW][2], int w, int lane, int m2, int nt, int k0, int nk, unsigned T, const u16* xq) {
  const int h = lane >> 5, q = w & 3;
  constexpr int WIN = NK < 8 ? NK : (TP ? 7 : 8);
  const unsigned hb = WS_HT + (m2 * NFB + k0) * 2048;
  v4u hf[WIN][2];
#pragma unroll
  for (int i = 0; i < WIN; ++i)
#pragma unroll
    for (int t = 0; t < 2; ++t) hf[i][t] = ld128(rws, (i + 1 < NK || nk == NK) ? lane * 16 : OOB, hb + (2 * i + t) * 1024, 1);
  SCHED();
  if (TP) wait_word(L + L_ZCT, 4);
  const unsigned zfo = WS_ZF + (32 * nt + 8 * q + (lane & 7)) * FL;
  v2u zf = {0, 0};
  v4u xv[2];
  v2u st[2];
  f32x16 acc[2][2];
#pragma unroll
  for (int t = 0; t < 2; ++t)
#pragma unroll
    for (int e = 0; e < 2; ++e)
#pragma unroll
      for (int r = 0; r < 16; ++r) acc[t][e][r] = 0.f;
#pragma unroll
  for (int i = 0; i < NK; ++i) {
    if (TP && i == NK - 3) {
      zf = flag_get(ws, zfo);
#pragma unroll
      for (int t = 0; t < 2; ++t) {
        xv[t] = *(const v4u*)(xq + 32 * t * D);
        st[t] = __builtin_amdgcn_raw_buffer_load_b64(rws, WS_STATS + (64 * m2 + 32 * t + (lane & 31)) * 8, 0, SC1);
      }
    }
#pragma unroll
    for (int t = 0; t < 2; ++t)
#pragma unroll
      for (int e = 0; e < 2; ++e) acc[t][e] = mfma32(asb(wdf[i][e]), asb(hf[i % WIN][t]), acc[t][e]);
    if (i + WIN < NK) {
#pragma unroll
      for (int t = 0; t < 2; ++t) hf[i % WIN][t] = ld128(rws, (i + WIN + 1 < NK || nk == NK) ? lane * 16 : OOB, hb + (2 * (i + WIN) + t) * 1024, 1);
    }
    SCHED();
  }
  v4u zv[2];
  if (TP) {
    if (!flag_up(zf, T)) poll_flags<1>(ws, zfo, T);
    ORDER_BEHIND_POLL();
#pragma unroll
    for (int t = 0; t < 2; ++t) zv[t] = ld128(rws, WS_ZT + (64 * nt + 16 * q + (lane >> 2)) * 4096 + (64 * m2 + 32 * t + 8 * (lane & 3)) * 2, 0, 1);
    SCHED();
  }
#pragma unroll
  for (int t = 0; t < 2; ++t)
#pragma unroll
    for (int qq = 0; qq < 4; ++qq) {
      const v4u o = {pk2(acc[t][0][4 * qq], acc[t][0][4 * qq + 1]), pk2(acc[t][0][4 * qq + 2], acc[t][0][4 * qq + 3]),
                     pk2(acc[t][1][4 * qq], acc[t][1][4 * qq + 1]), pk2(acc[t][1][4 * qq + 2], acc[t][1][4 * qq + 3])};
      *(LAS v4u*)(L + L_RED + w * 8192 + (32 * t + (lane & 31)) * 128 + (((2 * qq + h) ^ (lane & 7)) << 4)) = o;
    }
  SCHED();
  if (TP) {
#pragma unroll
    for (int t = 0; t < 2; ++t) *(LAS v4u*)(L + L_ZS + (2 * w + t) * 1024 + lane * 16) = zv[t];
#pragma unroll
    for (int t = 0; t < 2; ++t) {
      const float m = u2f(st[t].x), r = u2f(st[t].y);
      LAS char* zp = L + L_ZS + (2 * w + t) * 1024 + (8 * h + ((lane & 15) >> 2)) * 64 + (16 * ((lane >> 4) & 1) + 4 * (lane & 3)) * 2;
      const v2u z0 = __builtin_bit_cast(v2u, __builtin_amdgcn_ds_read_tr16_b64_v4i16((LAS s16x4*)(zp)));
      const v2u z1 = __builtin_bit_cast(v2u, __builtin_amdgcn_ds_read_tr16_b64_v4i16((LAS s16x4*)(zp + 256)));
      const unsigned zz[4] = {z0.x, z0.y, z1.x, z1.y};
      v4u o;
#pragma unroll
      for (int e = 0; e < 4; ++e) {
        const unsigned c = pk2(ssub(bflo(xv[t][e]), m), ssub(bfhi(xv[t][e]), m));
        const unsigned xn = pk2(smul(bflo(c), r), smul(bfhi(c), r));
        const unsigned a = pk2(smul(bflo(xn), bflo(zz[e])), smul(bfhi(xn), bfhi(zz[e])));
        o[e] = pk2(sadd(bflo(xv[t][e]), bflo(a)), sadd(bfhi(xv[t][e]), bfhi(a)));
      }
      *(LAS v4u*)(L + L_TP + (32 * t + (lane & 31)) * 128 + (((2 * q + h) ^ (lane & 7)) << 4)) = o;
    }
  }
}

__global__ __launch_bounds__(512, 2) void fused_kernel(const u16* __restrict__ x, const u16* __restrict__ wg, const u16* __restrict__ wu,
                                                            char* ws, const u16* __restrict__ kin, const u16* __restrict__ vin,
                                                            const u16* __restrict__ sk, const u16* __restrict__ hk, const u16* __restrict__ sD,
                                                            const u16* __restrict__ wd, u16* __restrict__ out) {
  __shared__ __attribute__((aligned(16))) char smem[LDS_BYTES];
  LAS char* L = (LAS char*)smem;
  const int tid = threadIdx.x, lane = tid & 63, w = __builtin_amdgcn_readfirstlane(tid >> 6), b = blockIdx.x;
  const int xcd = b & 7, j = b >> 3;
  const int idx = (xcd >> 1) * 28 + j;
  const int ft = j < 28 ? idx >> 3 : 14 + ((j - 28) >> 1);
  const int mt = (xcd & 1) * 8 + (j < 28 ? idx & 7 : (xcd & 6) + (j & 1));
  const int nblk = ft < 14 ? 6 : (ft == 14 ? 2 : 0);
  unsigned vx[2], vw[3];
#pragma unroll
  for (int p = 0; p < 2; ++p) vx[p] = (lane >> 3) * 1024 + (((lane & 7) ^ (4 * p + (lane >> 4))) << 4);
#pragma unroll
  for (int n = 0; n < 3; ++n) {
    const int G = 4 * n + (lane >> 4), kq = (G * 86) >> 8, uc = G - 3 * kq;
    const unsigned v = (kq * 4 + ((lane >> 2) & 3)) * WROW + uc * 64 + (lane & 3) * 16;
    vw[n] = 2 * uc < nblk ? v : OOB;
  }
  asm volatile("" ::"s"(x), "s"(wg), "s"(wu), "s"(ws), "s"(kin), "s"(vin), "s"(sk), "s"(hk), "s"(sD), "s"(wd), "s"(out));
  const rsrc_t rxt = mkrs(x, S * D * 2), rg = mkrs(wg, D * WROW), ru = mkrs(wu, D * WROW);
  auto stage = [&](int s) AI {
    if (nblk == 0) return;
    LAS char* dst = L + (s % 3) * SLOT;
    const unsigned sw = s * 64 * WROW + ft * 192;
#pragma unroll
    for (int i = 0; i < 16; ++i) dma(rxt, dst + i * 1024, vx[i & 1], (mt * 128 + 8 * i) * 1024 + s * 128);
#pragma unroll
    for (int n = 0; n < 12; ++n) dma(rg, dst + L_XW + n * 1024, vw[n % 3], sw + (n / 3) * 16 * WROW);
#pragma unroll
    for (int n = 0; n < 12; ++n) dma(ru, dst + L_UP + n * 1024, vw[n % 3] ^ 32, sw + (n / 3) * 16 * WROW);
  };
  if (w == 7) {
    unsigned long long pc;
    unsigned len;
    asm volatile("s_getpc_b64 %0\nfk_begin:\n\ts_mov_b32 %1, fk_end-fk_begin" : "=s"(pc), "=s"(len));
    const unsigned o = (unsigned)(j * 16 + lane) * 128u;
    if (lane < 16 && (int)o < (int)len) __builtin_amdgcn_global_load_lds((const GAS unsigned*)(pc + o), (LAS unsigned*)(L + L_UR0), 4, 0, 0);
  }
  if (w == 0 && lane < 32) lds_set(L + L_CTL + 4 + 4 * lane, 0u);
  BAR();
  v4u skv;
  if (w < 4) {
    const rsrc_t rk = mkrs(kin, S * D * 2), rv = mkrs(vin, S * D * 2);
    const int r8 = lane >> 3, p8 = lane & 7;
#pragma unroll
    for (int q = 0; q < 2; ++q) {
      const int i = 2 * w + q, row = 64 * j - 3 + 8 * i + r8;
      dma(rk, L + L_KS + i * 1024, row >= 0 ? row * 1024 + 128 * xcd + ((p8 ^ i) << 4) : OOB, 0);
      dma(rv, L + L_VS + i * 1024, r8 * 1024 + ((p8 ^ i) << 4), (64 * j + 8 * i) * 1024 + 128 * xcd);
    }
    if (w == 0) dma(rk, L + L_KS + 8192, r8 < 3 ? (64 * j + 61 + r8) * 1024 + 128 * xcd + (p8 << 4) : OOB, 0);
    skv = *(const v4u*)(sk + (64 * xcd + 16 * w + 2 * r8) * 4);
  }
  if (w == 4) { stage(0); lds_set(L + L_ORD, 1u); }
  const int m2 = (xcd & 1) * 16 + (xcd >> 1) * 4 + (j & 3), nt = j >> 2;
  const rsrc_t rws = mkrs(ws, WS_END);
  const int h = lane >> 5;
  constexpr int KB = (NFB - 4 * KA + 3) / 4, KBS = 4 * KB - (NFB - 4 * KA);
  const int nk = w < 4 ? KA : (w < 8 - KBS ? KB : KB - 1);
  const int k0 = w < 4 ? KA * w : 4 * KA + KB * (w - 4) - (w > 8 - KBS ? w - (8 - KBS) : 0);
  v4u wdf[KA][2];
  unsigned T;
  const unsigned hfo = WS_HF + (m2 * 32 + umin(k0 / 3 + (lane & 7), (k0 + nk - 1) / 3)) * FL;
  const u16* xq = x + (64 * m2 + (lane & 31)) * D + 64 * nt + 16 * (w & 3) + 8 * h;

  if (w < 4) {
    const int wm = w & 1, wn = w >> 1;
    f32x16 acc[2][3];
#pragma unroll
    for (int t = 0; t < 2; ++t)
#pragma unroll
      for (int i = 0; i < 3; ++i)
#pragma unroll
        for (int r = 0; r < 16; ++r) acc[t][i][r] = 0.f;
#pragma unroll
    for (int t = 0; t < 2; ++t)
#pragma unroll
      for (int i = 0; i < 3; ++i) asm volatile("" : "+v"(acc[t][i]));
    const int tok = 64 * wm + (lane & 31);
    const unsigned xa0 = tok * 128 + ((h ^ ((tok >> 1) & 7)) << 4);
    unsigned wb[3];
    {
      const int mat = (lane >> 4) & 1, q = (lane & 15) >> 2, p = lane & 3;
#pragma unroll
      for (int i = 0; i < 3; ++i) {
        const int fb = 3 * wn + i;
        wb[i] = L_XW + mat * 12288 + h * 1536 + q * 64 + p * 8 + (fb >> 1) * 256 + (((fb & 1) ^ mat) << 5);
      }
    }
    bf16x8 xf[2][2], wf[2][3];
    unsigned xs[3][4], wsl[3][3];
#pragma unroll
    for (int sl = 0; sl < 3; ++sl) {
#pragma unroll
      for (int ks = 0; ks < 4; ++ks) { xs[sl][ks] = (xa0 ^ (ks << 5)) + sl * SLOT; asm volatile("" : "+v"(xs[sl][ks])); }
#pragma unroll
      for (int i = 0; i < 3; ++i) { wsl[sl][i] = wb[i] + sl * SLOT; asm volatile("" : "+v"(wsl[sl][i])); }
    }
    auto rdw = [&](int sl, int ks, int i) AI -> bf16x8 {
      LAS char* wp = L + wsl[sl][i] + ks * 3072;
      s16x4 lo = __builtin_amdgcn_ds_read_tr16_b64_v4i16((LAS s16x4*)(wp));
      s16x4 hi = __builtin_amdgcn_ds_read_tr16_b64_v4i16((LAS s16x4*)(wp + 768));
      return __builtin_shufflevector(lo, hi, 0, 1, 2, 3, 4, 5, 6, 7);
    };
    auto ldfr = [&](int sl, int ks, int buf) AI {
      LAS char* xp = L + xs[sl][ks];
      xf[buf][0] = *(LAS bf16x8*)(xp);
      wf[buf][0] = rdw(sl, ks, 0);
      xf[buf][1] = *(LAS bf16x8*)(xp + 4096);
      wf[buf][1] = rdw(sl, ks, 1);
      wf[buf][2] = rdw(sl, ks, 2);
    };
    auto mm = [&](int buf, int lo, int hi) AI {
#pragma unroll
      for (int e = lo; e < hi; ++e) acc[e & 1][e >> 1] = mfma32(wf[buf][e >> 1], xf[buf][e & 1], acc[e & 1][e >> 1]);
    };
    {
      const int c = lane >> 3, sg = lane & 7;
      VMW(0);
      lds_add(L + L_STG, lane);
      wait_word(L + L_STG, 4);
      unsigned kl[11], vl[8];
      LAS char* kp = L + L_KS + sg * 1024 + (c & 3) * 4;
      const int ch = 2 * w + (c >> 2);
#pragma unroll
      for (int i = 0; i < 11; ++i) kl[i] = *(LAS unsigned*)(kp + i * 128 + ((ch ^ ((sg + (i >> 3)) & 7)) << 4));
#pragma unroll
      for (int i = 0; i < 8; ++i) vl[i] = *(LAS unsigned*)(kp + 9216 + i * 128 + ((ch ^ sg) << 4));
#pragma unroll
      for (int e = 0; e < 2; ++e) {
        const float w0 = bflo(skv[2 * e]), w1 = bfhi(skv[2 * e]), w2 = bflo(skv[2 * e + 1]), w3 = bfhi(skv[2 * e + 1]);
        float kf[11], r[8];
#pragma unroll
        for (int i = 0; i < 11; ++i) kf[i] = e ? bfhi(kl[i]) : bflo(kl[i]);
#pragma unroll
        for (int i = 0; i < 8; ++i)
          r[i] = __builtin_fmaf(kf[i + 3], w0, __builtin_fmaf(kf[i + 2], w1, __builtin_fmaf(kf[i + 1], w2, kf[i] * w3))) * (e ? bfhi(vl[i]) : bflo(vl[i]));
        *(LAS v4u*)(L + L_KO + (16 * w + 2 * c + e) * 128 + (7 - sg) * 16) = v4u{pk2(r[7], r[6]), pk2(r[5], r[4]), pk2(r[3], r[2]), pk2(r[1], r[0])};
      }
      lds_add(L + L_KVD, lane);
    }
    __builtin_amdgcn_s_setprio(2);
    if (nblk == 0) lds_set(L + L_FREE + 4 * w, 8u);
    if (b == 255 && w == 0) {
      wait_word(L + L_KCT, 1);
      const unsigned T = lds_get(L + L_CTL);
#pragma unroll 1
      for (int i = 0; i < 4; ++i) poll_flags<8>(ws, WS_PF + (64 * i + lane) * FL, T);
      if (lane == 0) word_set(ws, WS_EPOCH, T);
    }
    wait_word(L + L_FULL, 1);
    if (nblk) {
      ldfr(0, 0, 0);
      SCHED();
#define STEP(ks, nb, cb) ldfr(sl, ks, nb); mm(cb, 0, 6); \
      SGB(8, 1); SGB(0x100, 3); SGB(8, 1); SGB(0x100, 1); SGB(8, 1); SGB(0x100, 2); SGB(8, 1); SGB(0x100, 2); SGB(8, 2); SCHED();
#pragma unroll
      for (int s = 0; s < 8; ++s) {
        const int sl = s % 3;
        STEP(1, 1, 0) STEP(2, 0, 1)
        unsigned fl = 1;
        if (s < 7) fl = *(volatile LAS unsigned*)(L + L_FULL + 4 * (s + 1));
        SCHED();
        STEP(3, 1, 0)
        mm(1, 0, 1);
        lds_set(L + L_FREE + 4 * w, s + 1);
        SCHED();
        mm(1, 1, 2);
        SCHED();
        if (s < 7) {
          if (__builtin_expect(__builtin_amdgcn_readfirstlane(fl) == 0, 0)) wait_word(L + L_FULL + 4 * (s + 1), 1);
          asm volatile("" ::: "memory");
          const int ns = (s + 1) % 3;
          xf[0][0] = *(LAS bf16x8*)(L + xs[ns][0]);
          wf[0][0] = rdw(ns, 0, 0);
          SCHED();
          mm(1, 2, 3);
          SCHED();
          xf[0][1] = *(LAS bf16x8*)(L + xs[ns][0] + 4096);
          wf[0][1] = rdw(ns, 0, 1);
          SCHED();
          mm(1, 3, 4);
          SCHED();
          wf[0][2] = rdw(ns, 0, 2);
          SCHED();
        } else {
          mm(1, 2, 4);
        }
        mm(1, 4, 6);
        SCHED();
      }
    }
    wait_word(L + L_EPI, 1);
    wait_word(L + L_POK, 2);
    T = lds_get(L + L_CTL);
    {
      const int mt64 = 2 * mt + wm;
      f32x2 nm[2], K1[2], K2[2], iR2[2];
#pragma unroll
      for (int t = 0; t < 2; ++t) {
        const f32x2 st = *(LAS f32x2*)(L + L_ST + (tok + 32 * t) * 8);
        const float r = st[1], k1 = -2.3022082f * r, k2 = (-2.3022082f * 0.044715f) * r * r * r, ir = __builtin_amdgcn_rcpf(r * r);
        nm[t] = f32x2{-st[0], -st[0]}; K1[t] = f32x2{k1, k1}; K2[t] = f32x2{k2, k2}; iR2[t] = f32x2{ir, ir};
      }
#pragma unroll
      for (int i = 0; i < 3; ++i) {
        LAS char* cp = L + L_CS + (16 * (3 * wn + i) + 4 * h) * 4;
        const f32x4 cg0 = *(LAS f32x4*)(cp), cg1 = *(LAS f32x4*)(cp + 32), cu0 = *(LAS f32x4*)(cp + 384), cu1 = *(LAS f32x4*)(cp + 416);
        const unsigned vo = 3 * wn + i < nblk ? lane * 16 : OOB;
#pragma unroll
        for (int t = 0; t < 2; ++t) {
          v4u o;
#pragma unroll
          for (int e = 0; e < 4; ++e) {
            const f32x4 cgv = e < 2 ? cg0 : cg1, cuv = e < 2 ? cu0 : cu1;
            const f32x2 cg = {cgv[2 * (e & 1)], cgv[2 * (e & 1) + 1]}, cu = {cuv[2 * (e & 1)], cuv[2 * (e & 1) + 1]};
            const f32x2 ag = {acc[t][i][2 * e], acc[t][i][2 * e + 1]}, au = {acc[t][i][8 + 2 * e], acc[t][i][9 + 2 * e]};
            const f32x2 G = __builtin_elementwise_fma(nm[t], cg, ag), U = __builtin_elementwise_fma(nm[t], cu, au);
            const f32x2 ww = G * __builtin_elementwise_fma(G * G, K2[t], K1[t]);
            const f32x2 ex = {__builtin_amdgcn_exp2f(ww[0]), __builtin_amdgcn_exp2f(ww[1])};
            const f32x2 dn = __builtin_elementwise_fma(ex, iR2[t], iR2[t]);
            const f32x2 rc = {__builtin_amdgcn_rcpf(dn[0]), __builtin_amdgcn_rcpf(dn[1])};
            const f32x2 hv = (G * U) * rc;
            o[e] = pk2(hv[0], hv[1]);
            {
              const int qq = (2 * i + t) * 4 + e;
              wdf[qq >> 1][qq & 1] = ld128(rws, lane * 16, WS_WDT + ((nt * NFB + k0) * 2 + qq) * 1024, 1);
              SCHED();
            }
          }
          st128(o, rws, vo, WS_HT + ((mt64 * NFB + ft * 6 + 3 * wn + i) * 2 + t) * 1024);
        }
      }
      lds_set(L + L_FREE + 4 * w, 9u);
      VMW(0);
      flag_set(rws, WS_HF + (mt64 * 32 + ft * 2 + wn) * FL, T, lane, 3 * wn < nblk);
    }
    __builtin_amdgcn_s_setprio(0);
    SCHED();
    wait_free(L, 8);
    poll_flags<2>(ws, hfo, T);
    gemm2_part<KA, KA, true>(L, ws, rws, wdf, w, lane, m2, nt, k0, nk, T, xq);
  } else {
    const int chl = (w - 4) >> 1, d0 = 64 * xcd + 2 * j, d = d0 + chl;
    const int m0 = 1 + (w & 1) * 128 + lane;
    v4u hl[2][2];
    auto load_h = [&]() AI {
      const rsrc_t rh = mkrs(hk + d * S, S * 2);
#pragma unroll
      for (int i = 0; i < 2; ++i) { hl[i][0] = ld128(rh, (m0 + 64 * i - 2) * 16, 0, 0); hl[i][1] = ld128(rh, (m0 + 64 * i - 1) * 16, 0, 0); }
      SCHED();
    };
    v4u pl[16];
    auto load_wd = [&]() AI {
#pragma unroll
      for (int i = 0; i < KB; ++i)
#pragma unroll
        for (int e = 0; e < 2; ++e) wdf[i][e] = ld128(rws, i < nk ? lane * 16 : OOB, WS_WDT + ((nt * NFB + k0 + i) * 2 + e) * 1024, 1);
      SCHED();
    };
    if (w < 6) {
      __builtin_amdgcn_s_setprio(3);
      if (w == 5) {
        wait_word(L + L_ORD, 1);
        stage(1);
        stage(2);
        if (nblk) VMW(40);
        lds_set(L + L_FULL + 4, 1u);
      }
      VMW(0);
      lds_set(L + L_FULL + 8 * (w - 4), 1u);
#pragma unroll 1
      for (int n = w - 1; n < 8; n += 2) {
        wait_free(L, n - 2);
        stage(n);
        lds_set(L + L_ISS + 4 * (n & 1), n);
        if (n >= 6) break;
        VMW(0);
        lds_set(L + L_FULL + 4 * n, 1u);
      }
      load_h();
      VMW(4);
      lds_set(L + L_FULL + 4 * (11 - w), 1u);
      __builtin_amdgcn_s_setprio(0);
    } else {
      const int hw = w - 6;
      unsigned E1 = 0;
      if (hw) {
        E1 = word_get(ws, WS_EPOCH) + 1u;
        const rsrc_t rw = mkrs(wd, F * D * 2);
#pragma unroll
        for (int u = 0; u < 3; ++u) {
          const int uid = b + 256 * u;
#pragma unroll
          for (int i = 0; i < 2; ++i)
            dma(rw, L + L_WS + (2 * u + i) * 1024, uid < 688 ? (lane >> 3) * 1024 + (lane & 7) * 16 : OOB, (16 * (uid >> 3) + 8 * i) * 1024 + (uid & 7) * 128);
        }
        const rsrc_t rx = mkrs(x, S * D * 2);
#pragma unroll
        for (int i = 0; i < 8; ++i) pl[i] = ld128(rx, (lane >> 3) * 1024 + (lane & 7) * 16, (mt * 128 + ft * 8) * 1024 + i * 128, 0);
        SCHED();
        lds_set(L + L_CTL, E1);
        lds_set(L + L_KCT, 1u);
      } else {
        const int c = lane >> 1, cc = c < 12 ? c : c - 12;
        const bool ok = lane < 48 && cc < 2 * nblk;
        const char* base = (const char*)(c < 12 ? wg : wu) + (ok ? (size_t)((32 * mt + 16 * (lane & 1)) * WROW + ft * 192 + cc * 16) : 0);
#pragma unroll
        for (int i = 0; i < 16; ++i) pl[i] = *(const v4u*)(base + i * WROW);
        SCHED();
      }
      __builtin_amdgcn_s_setprio(3);
      wait_word(L + L_KCT, 1);
      E1 = lds_get(L + L_CTL);
      if (hw) {
        LAS char* tp = L + L_WS + (4 * h + ((lane & 15) >> 2)) * 128 + (4 * ((lane >> 4) & 1) + (lane & 3)) * 16;
        VMW(8);
#pragma unroll
        for (int q = 0; q < 6; ++q) {
          const int uid = b + 256 * (q >> 1);
          const v2u lo = __builtin_bit_cast(v2u, __builtin_amdgcn_ds_read_tr16_b64_v4i16((LAS s16x4*)(tp + (q >> 1) * 2048 + (q & 1) * 8)));
          const v2u hi = __builtin_bit_cast(v2u, __builtin_amdgcn_ds_read_tr16_b64_v4i16((LAS s16x4*)(tp + (q >> 1) * 2048 + (q & 1) * 8 + 1024)));
          st128(v4u{lo.x, lo.y, hi.x, hi.y}, rws, uid < 688 ? lane * 16 : OOB, WS_WDT + (((uid & 7) * NFB + (uid >> 3)) * 2 + (q & 1)) * 1024);
        }
        float s0 = 0.f, s1 = 0.f;
#pragma unroll
        for (int i = 0; i < 8; ++i)
#pragma unroll
          for (int e = 0; e < 4; ++e) { s0 = sadd(s0, bflo(pl[i][e])); s1 = sadd(s1, bfhi(pl[i][e])); }
        const float mean = sum8(s0 + s1) * (1.0f / D);
        float q0 = 0.f, q1 = 0.f;
#pragma unroll
        for (int i = 0; i < 8; ++i)
#pragma unroll
          for (int e = 0; e < 4; ++e) {
            const float a0 = ssub(bflo(pl[i][e]), mean), a1 = ssub(bfhi(pl[i][e]), mean);
            q0 = sfma(a0, a0, q0); q1 = sfma(a1, a1, q1);
          }
        const float sd = rbf(__builtin_sqrtf(rbf(rbf(sum8(q0 + q1) * (1.0f / D)) + 1e-5f)));
        st64(v2u{f2u(rbf(mean)), f2u(1.0f / sd)}, rws, (lane & 7) == 0 ? WS_STATS + (mt * 128 + ft * 8 + (lane >> 3)) * 8 : OOB, 0);
      } else {
        wait_word(L + L_KVD, 4);
#pragma unroll
        for (int i = 0; i < 8; ++i)
          st128(*(LAS v4u*)(L + L_KO + i * 1024 + lane * 16), rws, WS_KVR + (64 * xcd + (lane >> 3)) * 4096 + (1984 - 64 * j) * 2 + (lane & 7) * 16, i * 8 * 4096);
        SCHED();
        float cs[8];
#pragma unroll
        for (int e = 0; e < 8; ++e) cs[e] = 0.f;
#pragma unroll
        for (int i = 0; i < 16; ++i)
#pragma unroll
          for (int e = 0; e < 4; ++e) { cs[2 * e] = sadd(cs[2 * e], bflo(pl[i][e])); cs[2 * e + 1] = sadd(cs[2 * e + 1], bfhi(pl[i][e])); }
        float o[4];
#pragma unroll
        for (int e = 0; e < 4; ++e) {
          const float mine = (lane & 1) ? cs[4 + e] : cs[e], give = (lane & 1) ? cs[e] : cs[4 + e];
          o[e] = mine + dpp<0xB1>(give);
        }
        st128(v4u{f2u(o[0]), f2u(o[1]), f2u(o[2]), f2u(o[3])}, rws, (lane < 48 && nblk) ? lane * 16 : OOB, WS_CSP + (ft * 16 + mt) * 768);
      }
      __builtin_amdgcn_s_setprio(0);
      arrive(L, L_PCT, 2, rws, WS_PF + b * FL, E1, lane);
      {
        const unsigned po = WS_PF + (hw ? (lane < 16 ? wg_of_tile(mt, lane) : wg_of_tile(lane & 15, ft)) : (lane & 31) * 8 + (lane < 32 ? xcd : nt)) * FL;
        bool ok = false;
#pragma unroll 1
        for (int n = 6; n < 8 && !ok; ++n) {
          wait_word(L + L_ISS + 4 * (n & 1), n);
          ok = flag_up(flag_get(ws, po), E1);
        }
        if (!ok) poll_flags<4>(ws, po, E1);
        ORDER_BEHIND_POLL();
      }
      if (hw) {
#pragma unroll
        for (int i = 0; i < 16; ++i) pl[i] = ld128(rws, (lane < 48 && nblk) ? lane * 16 : OOB, WS_CSP + (ft * 16 + i) * 768, 1);
        const v4u sv = ld128(rws, lane * 16, WS_STATS + mt * 1024, 1);
        load_h();
        SCHED();
        lds_add(L + L_POK, lane);
        f32x4 cs = {0.f, 0.f, 0.f, 0.f};
#pragma unroll
        for (int i = 0; i < 16; ++i)
#pragma unroll
          for (int e = 0; e < 4; ++e) cs[e] = sadd(cs[e], u2f(pl[i][e]));
        if (lane < 48) *(LAS f32x4*)(L + L_CS + lane * 16) = cs;
        *(LAS v4u*)(L + L_ST + lane * 16) = sv;
        lds_set(L + L_EPI, 1u);
      } else {
#pragma unroll
        for (int i = 0; i < 8; ++i) pl[i] = ld128(rws, lane * 16, WS_KVR + d0 * 4096 + i * 1024, 1);
        load_h();
        SCHED();
        lds_add(L + L_POK, lane);
      }
    }
    LAS char* hc = L + (chl ? L_HC1 : L_HC0);
    LAS char* ur = L + (chl ? L_UR1 : L_UR0);
    const float Dd = bflo(sD[d]);
    wait_word(L + L_POK, 2);
    wait_word(L + L_ISS + 4, 7);
    asm volatile("" : "+v"(hl[0][0]), "+v"(hl[0][1]), "+v"(hl[1][0]), "+v"(hl[1][1]));
    if (w == 6) {
#pragma unroll
      for (int i = 0; i < 8; ++i) asm volatile("" : "+v"(pl[i]));
    }
    load_wd();
    wait_word(L + L_FULL + 28, 1);
    wait_word(L + L_PCT, 2);
    wait_free(L, 6);
    build_hc(hc, m0, hl[0][0], hl[0][1]);
    build_hc(hc, m0 + 64, hl[1][0], hl[1][1]);
    if (w & 1) {
      if (lane < 8) *(LAS v4u*)(hc + lane * HC_P) = v4u{0, 0, 0, 0};
      else if (lane < 40) *(LAS v4u*)(ur + 4096 + (lane - 8) * 16) = v4u{0, 0, 0, 0};
    }
    if (w == 6) {
#pragma unroll
      for (int i = 0; i < 8; ++i) *(LAS v4u*)(L + (i < 4 ? L_UR0 : L_UR1 - 4096) + i * 1024 + lane * 16) = pl[i];
    }
    lds_add(L + L_CVK, lane);
    wait_word(L + L_CVK, 4);
    wait_free(L, 8);
    T = lds_get(L + L_CTL);
    v2u hv;
    LAS char* xch = L + L_XCH + chl * 1024;
    LAS char* xfl = L + L_XFL + chl * 4;
    if (w & 1) hv = conv_part<true>(hc, ur, xch, xfl, L + L_FREE + 4 * (w - 4), lane, Dd, rws, d, ws, hfo);
    else hv = conv_part<false>(hc, ur, xch, xfl, L + L_FREE + 4 * (w - 4), lane, Dd, rws, d, ws, hfo);
    __builtin_amdgcn_s_setprio(0);
    asm volatile("" ::: "memory");
    arrive(L, L_ZCT, 4, rws, WS_ZF + (d0 >> 1) * FL, T, lane);
    if (!flag_up(hv, T)) poll_flags<2>(ws, hfo, T);
    ORDER_BEHIND_POLL();
    gemm2_part<KB, KA, false>(L, ws, rws, wdf, w, lane, m2, nt, k0, nk, T, xq);
  }
  BAR();
  {
    {
      LAS char* rp = L + L_RED + w * 1024 + (lane >> 3) * 128 + (((lane & 7) ^ (lane >> 3)) << 4);
      float f[8];
#pragma unroll
      for (int e = 0; e < 8; ++e) f[e] = 0.f;
      v4u p[8];
#pragma unroll
      for (int ww = 0; ww < 8; ++ww) p[ww] = *(LAS v4u*)(rp + ww * 8192);
      const v4u tp = *(LAS v4u*)(rp + L_TP);
#pragma unroll
      for (int ww = 0; ww < 8; ++ww)
#pragma unroll
        for (int e = 0; e < 4; ++e) {
          f[2 * e] += bflo(p[ww][e]); f[2 * e + 1] += bfhi(p[ww][e]);
        }
      v4u o;
#pragma unroll
      for (int e = 0; e < 4; ++e) {
        const unsigned ff = pk2(f[2 * e], f[2 * e + 1]);
        o[e] = pk2(bflo(tp[e]) + bflo(ff), bfhi(tp[e]) + bfhi(ff));
      }
      __builtin_amdgcn_raw_buffer_store_b128(o, mkrs(out, S * D * 2), ((64 * m2 + 8 * w + (lane >> 3)) * D + 64 * nt + 8 * (lane & 7)) * 2, 0, SC1);
    }
    asm volatile("fk_end:");
  }
}

extern "C" void kernel_launch(void* const* d_in, const int* in_sizes, int n_in, void* d_out, int out_size, void* d_ws, size_t ws_size,
                              hipStream_t stream) {
  fused_kernel<<<256, 512, 0, stream>>>((const u16*)d_in[0], (const u16*)d_in[7], (const u16*)d_in[8], (char*)d_ws, (const u16*)d_in[1],
                                        (const u16*)d_in[2], (const u16*)d_in[3], (const u16*)d_in[4], (const u16*)d_in[5], (const u16*)d_in[9],
                                        (u16*)d_out);
}
```
